```python
import torch

def _softmax(x, axis=-1):
    return torch.softmax(x, dim=axis)
def _gelu(x):
    return torch.nn.functional.gelu(x, approximate="tanh")
def _silu(x):
    return torch.nn.functional.silu(x)
def _sigmoid(x):
    return torch.sigmoid(x)
def _layernorm(x, axis=-1, eps=1e-5):
    m = x.mean(dim=axis, keepdim=True)
    v = x.var(dim=axis, keepdim=True, unbiased=False)
    return (x - m) / torch.sqrt(v + eps)
def _scatter_add(tgt, idx, src, axis=0):
    return tgt.clone().index_add_(axis, idx, src)
def _segment_sum(src, seg, n):
    out = torch.zeros((n,) + src.shape[1:], dtype=src.dtype)
    return out.index_add_(0, seg, src)
def _pad(x, pw):
    flat = tuple(v for pair in reversed(pw) for v in pair)
    return torch.nn.functional.pad(x, flat)

kernel_name = 'gated_mix__grouped_kv_mqa__mixer_channel_mlp__prompt'

def setup_inputs():

    _w = (lambda a: (lambda t: t.to(torch.bfloat16) if t.is_floating_point() else t)(torch.as_tensor(a)))
    def _A_setup():
        B, S, D, H = 1, 2048, 576, 32
        Dh = D // H
        return {
            "x": _w((lambda *s: torch.randn(*s, dtype=torch.bfloat16) * 0.05)(B, S, D)),
            "wq": _w((lambda *s: torch.randn(*s, dtype=torch.bfloat16) * 0.8)(D, H * Dh)),
            "wk": _w((lambda *s: torch.randn(*s, dtype=torch.bfloat16) * 0.8)(D, Dh)),
            "wv": _w((lambda *s: torch.randn(*s, dtype=torch.bfloat16) * 0.2)(D, Dh)),
            "wo": _w((lambda *s: torch.randn(*s, dtype=torch.bfloat16) * 0.2)(H * Dh, D)),
        }
    _A_in = _A_setup()

    def _B_setup():
        B, S, D, F = 1, 2048, 576, 4608
        return {
            "x": _w((lambda *s: torch.randn(*s, dtype=torch.bfloat16) * 0.05)(B, S, D)),
            "w1": _w((lambda *s: torch.randn(*s, dtype=torch.bfloat16) * 0.8)(D, F) * (D ** -0.5)),
            "b1": _w((lambda *s: torch.randn(*s, dtype=torch.bfloat16) * 0.8)(F)),
            "w2": _w((lambda *s: torch.randn(*s, dtype=torch.bfloat16) * 0.05)(F, D) * (F ** -0.5)),
            "b2": _w((lambda *s: torch.randn(*s, dtype=torch.bfloat16) * 0.05)(D)),
        }
    _B_in = _B_setup()

    return {
        "A_x": _A_in["x"],
        "A_wq": _A_in["wq"],
        "A_wk": _A_in["wk"],
        "A_wv": _A_in["wv"],
        "A_wo": _A_in["wo"],
        "B_x": _B_in["x"],
        "B_w1": _B_in["w1"],
        "B_b1": _B_in["b1"],
        "B_w2": _B_in["w2"],
        "B_b2": _B_in["b2"],
        "gate_w": _w((lambda *s: torch.randn(*s, dtype=torch.bfloat16) * 0.05)(576) * 16.0),
    }

def reference(A_x, A_wq, A_wk, A_wv, A_wo, B_x, B_w1, B_b1, B_w2, B_b2, gate_w):
    def _A_ref(x, wq, wk, wv, wo):
        B, S, D = x.shape
        H = 32
        Dh = D // H
        q = (lambda x, *d: x.permute(*d))((x @ wq).reshape(B, S, H, Dh), 0, 2, 1, 3)
        k = x @ wk
        v = x @ wv
        scores = torch.einsum("bhqd,bkd->bhqk", q, k) * (Dh ** -0.5)
        p = _softmax(scores, axis=-1)
        o = torch.einsum("bhqk,bkd->bhqd", p, v)
        return (lambda x, *d: x.permute(*d))(o, 0, 2, 1, 3).reshape(B, S, H * Dh) @ wo

    def _B_ref(x, w1, b1, w2, b2):
        y = _layernorm(x, axis=-1)
        h = _gelu(torch.einsum("bsd,df->bsf", y, w1) + b1)
        return torch.einsum("bsf,fd->bsd", h, w2) + b2

    x = A_x
    g = _sigmoid((x * gate_w).sum(-1, keepdims=True))
    a = _A_ref(x, A_wq, A_wk, A_wv, A_wo)
    b = _B_ref(x, B_w1, B_b1, B_w2, B_b2)
    return g * a + (1 - g) * b
```

```cpp
#include <hip/hip_runtime.h>
#include <hip/hip_bf16.h>

typedef __bf16 bf16x8 __attribute__((ext_vector_type(8)));
typedef __bf16 bf16x16 __attribute__((ext_vector_type(16)));
typedef short s16x4 __attribute__((ext_vector_type(4)));
typedef float f32x4 __attribute__((ext_vector_type(4)));
typedef float f32x8 __attribute__((ext_vector_type(8)));
typedef float f32x16 __attribute__((ext_vector_type(16)));
typedef unsigned u32x2 __attribute__((ext_vector_type(2)));
typedef unsigned u32x4 __attribute__((ext_vector_type(4)));
#define LAS __attribute__((address_space(3)))
#define GAS __attribute__((address_space(1)))
#define DI __device__ __forceinline__
#define SB() __builtin_amdgcn_sched_barrier(0)

constexpr unsigned WS_EPOCH = 0;
constexpr unsigned WS_KVFLAG = 256;
constexpr unsigned WS_PFLAG = 1024;
constexpr unsigned WS_OFLAG = 4096;
constexpr unsigned WS_KNORM = 8192;
constexpr unsigned WS_STAMP = 16384;
constexpr unsigned WS_KV = 262144;
constexpr unsigned WS_O = 524288;
constexpr unsigned WS_PART = 4194304;
constexpr unsigned WS_END = WS_PART + 64u * 18u * 16u * 2048u;

constexpr unsigned L_H = 73728;
constexpr unsigned L_KVST = L_H + 61440;
constexpr unsigned L_RING = 110592;
constexpr unsigned L_M = 147456;
constexpr unsigned L_QR = L_M;
constexpr unsigned L_SSQ = L_M + 10240;
constexpr unsigned L_SUM = L_M + 11264;
constexpr unsigned L_GL = L_M + 11776;
constexpr unsigned L_CS = L_M + 12288;
constexpr unsigned L_B1 = L_M + 13312;
constexpr unsigned L_B2 = L_M + 14336;
constexpr unsigned L_END = 163840;

struct Params {
  const void* x; const void* wq; const void* wk; const void* wv; const void* wo;
  const void* w1; const void* b1; const void* w2; const void* b2; const void* gw;
  void* out; void* ws;
};

DI u32x4 mkrs(const void* p, unsigned bytes) {
  u32x4 r = {(unsigned)(size_t)p, (unsigned)((size_t)p >> 32) & 0xffffu, bytes, 0x20000u};
  return r;
}
DI void glds(u32x4 rs, unsigned voff, unsigned soff, unsigned ldsb) {
  asm volatile("s_mov_b32 m0, %3\n\ts_nop 0\n\tbuffer_load_dwordx4 %0, %1, %2 offen lds"
               :: "v"(voff), "s"(rs), "s"(soff), "s"(ldsb) : "memory", "m0");
}
DI void glds_sc1(u32x4 rs, unsigned voff, unsigned soff, unsigned ldsb) {
  asm volatile("s_mov_b32 m0, %3\n\ts_nop 0\n\tbuffer_load_dwordx4 %0, %1, %2 offen sc1 lds"
               :: "v"(voff), "s"(rs), "s"(soff), "s"(ldsb) : "memory", "m0");
}
DI void glds_half_sc1(u32x4 rs, unsigned voff, unsigned soff, unsigned ldsb) {
  asm volatile("s_mov_b32 m0, %3\n\ts_mov_b32 exec_hi, 0\n\tbuffer_load_dwordx4 %0, %1, %2 offen sc1 lds\n\ts_mov_b32 exec_hi, -1"
               :: "v"(voff), "s"(rs), "s"(soff), "s"(ldsb) : "memory", "m0");
}
DI void glds_half(u32x4 rs, unsigned voff, unsigned soff, unsigned ldsb) {
  asm volatile("s_mov_b32 m0, %3\n\ts_mov_b32 exec_hi, 0\n\tbuffer_load_dwordx4 %0, %1, %2 offen lds\n\ts_mov_b32 exec_hi, -1"
               :: "v"(voff), "s"(rs), "s"(soff), "s"(ldsb) : "memory", "m0");
}
DI void glds_wq(u32x4 rs, unsigned voff, unsigned soff, unsigned ldsb) {
  asm volatile("s_mov_b32 m0, %3\n\ts_mov_b32 exec_lo, 0xf1f11f1f\n\ts_mov_b32 exec_hi, 0xf1f11f1f\n\t"
               "buffer_load_dwordx4 %0, %1, %2 offen lds\n\ts_mov_b64 exec, -1"
               :: "v"(voff), "s"(rs), "s"(soff), "s"(ldsb) : "memory", "m0");
}
DI void aload4(u32x4& d, u32x4 rs, unsigned voff, unsigned soff) {
  asm volatile("buffer_load_dwordx4 %0, %1, %2, %3 offen" : "=&v"(d) : "v"(voff), "s"(rs), "s"(soff) : "memory");
}
DI void aload1(unsigned& d, u32x4 rs, unsigned voff, unsigned soff) {
  asm volatile("buffer_load_dword %0, %1, %2, %3 offen" : "=&v"(d) : "v"(voff), "s"(rs), "s"(soff) : "memory");
}
DI void aload1_sc1(unsigned& d, u32x4 rs, unsigned voff, unsigned soff) {
  asm volatile("buffer_load_dword %0, %1, %2, %3 offen sc1" : "=&v"(d) : "v"(voff), "s"(rs), "s"(soff) : "memory");
}
DI void aload4_sc1(u32x4& d, u32x4 rs, unsigned voff, unsigned soff) {
  asm volatile("buffer_load_dwordx4 %0, %1, %2, %3 offen sc1" : "=&v"(d) : "v"(voff), "s"(rs), "s"(soff) : "memory");
}
DI void store4_sc1(u32x4 d, u32x4 rs, unsigned voff, unsigned soff) {
  asm volatile("buffer_store_dwordx4 %0, %1, %2, %3 offen sc1\n\ts_nop 1" :: "v"(d), "v"(voff), "s"(rs), "s"(soff) : "memory");
}
DI void store1_sc1(unsigned d, u32x4 rs, unsigned voff, unsigned soff) {
  asm volatile("buffer_store_dword %0, %1, %2, %3 offen sc1" :: "v"(d), "v"(voff), "s"(rs), "s"(soff) : "memory");
}
#define TIE(v) asm volatile("" : "+v"(v))
template <int N> DI void wait_vm() { asm volatile("s_waitcnt vmcnt(%0)" :: "n"(N) : "memory"); }
template <int N> DI void sync_vm() { asm volatile("s_waitcnt vmcnt(%0) lgkmcnt(0)\n\ts_barrier" :: "n"(N) : "memory"); }
DI void sync_lds() { asm volatile("s_waitcnt lgkmcnt(0)\n\ts_barrier" ::: "memory"); }

DI unsigned cvtpk(float a, float b) { unsigned r; asm("v_cvt_pk_bf16_f32 %0, %1, %2" : "=v"(r) : "v"(a), "v"(b)); return r; }
DI float bflo(unsigned u) { return __uint_as_float(u << 16); }
DI float bfhi(unsigned u) { return __uint_as_float(u & 0xffff0000u); }
DI float rbf(float a) { return bflo(cvtpk(a, a)); }
DI float swap32(float v) { return __shfl_xor(v, 32); }

DI bf16x8 trfrag(LAS char* sm, unsigned a0, unsigned a1) {
  s16x4 lo = __builtin_amdgcn_ds_read_tr16_b64_v4i16((LAS s16x4*)(sm + a0));
  s16x4 hi = __builtin_amdgcn_ds_read_tr16_b64_v4i16((LAS s16x4*)(sm + a1));
  u32x2 l = __builtin_bit_cast(u32x2, lo), h = __builtin_bit_cast(u32x2, hi);
  u32x4 r = {l[0], l[1], h[0], h[1]};
  return __builtin_bit_cast(bf16x8, r);
}
DI bf16x8 ldfrag(LAS char* sm, unsigned a) { return *(LAS bf16x8*)(sm + a); }
DI f32x16 mfma32(bf16x8 a, bf16x8 b, f32x16 c) { return __builtin_amdgcn_mfma_f32_32x32x16_bf16(a, b, c, 0, 0, 0); }
DI f32x4 mfma16(bf16x8 a, bf16x8 b, f32x4 c) { return __builtin_amdgcn_mfma_f32_16x16x32_bf16(a, b, c, 0, 0, 0); }

DI void spin_flags(const void* ws, unsigned off, bool pred, unsigned tag) {
  GAS unsigned* p = (GAS unsigned*)((GAS char*)ws + off);
  for (unsigned it = 0; it < (1u << 22); ++it) {
    unsigned v = pred ? __hip_atomic_load(p, __ATOMIC_RELAXED, __HIP_MEMORY_SCOPE_AGENT) : tag;
    if (__all(v == tag)) break;
    __builtin_amdgcn_s_sleep(1);
  }
}
DI void set_flag(u32x4 rs, unsigned off, unsigned tag) {
  unsigned z = 0, t = tag;
  asm volatile("s_mov_b64 exec, 1\n\tbuffer_store_dword %0, %1, %2, %3 offen sc1\n\ts_mov_b64 exec, -1"
               :: "v"(t), "v"(z), "s"(rs), "s"(off) : "memory");
}
#define STAMP(i)
#define STAMPC(i)
#define ABL 0

template <class T> DI T lds_ld(LAS char* sm, unsigned a) { return *(LAS T*)(sm + a); }
template <class T> DI void lds_st(LAS char* sm, unsigned a, T v) { *(LAS T*)(sm + a) = v; }
DI bf16x8 asfrag(u32x4 v) { return __builtin_bit_cast(bf16x8, v); }
DI unsigned add64k(unsigned a) { unsigned r; asm volatile("v_add_u32 %0, 0x10000, %1" : "=v"(r) : "v"(a)); return r; }
DI f32x16 zero16() { f32x16 z; for (int i = 0; i < 16; ++i) z[i] = 0.f; return z; }

struct Gelu {
  float k1, k2, ex, y0;
  unsigned hp0, hp1, hp2, hp3;
};
template <int S> DI void gelu_step(Gelu& g, const f32x16& pa, LAS char* sm, unsigned haddr) {
  if constexpr (S >= 2 && S < 34) {
    constexpr int e = (S - 2) >> 1;
    const float a = pa[e];
    if constexpr ((S & 1) == 0) {
      float t = __builtin_fmaf(g.k2, a * a, g.k1);
      g.ex = __builtin_amdgcn_exp2f(a * t);
    } else {
      float y = a * __builtin_amdgcn_rcpf(1.f + g.ex);
      if constexpr ((e & 1) == 0) g.y0 = y;
      else {
        unsigned pk = cvtpk(g.y0, y);
        if constexpr ((e & 7) == 1) g.hp0 = pk;
        if constexpr ((e & 7) == 3) g.hp1 = pk;
        if constexpr ((e & 7) == 5) g.hp2 = pk;
        if constexpr ((e & 7) == 7) { u32x4 v = {g.hp0, g.hp1, g.hp2, pk}; lds_st<u32x4>(sm, haddr + (e >> 3) * 1024, v); }
      }
    }
  }
}
template <int S, int E> struct Unroll {
  template <class F> static DI void run(F&& f) { f.template operator()<S>(); Unroll<S + 1, E>::run(f); }
};
template <int E> struct Unroll<E, E> { template <class F> static DI void run(F&&) {} };

DI bf16x8 make_aext(float cs, unsigned b1u) {
  unsigned hh = cvtpk(cs, cs);
  float lo = cs - bflo(hh);
  unsigned bb = b1u | (b1u << 16);
  unsigned lb = (cvtpk(lo, lo) & 0xffffu) | (b1u << 16);
  u32x4 r = {hh, lb, bb, 0u};
  return asfrag(r);
}

__global__ __launch_bounds__(512, 2) void fused_kernel(Params P) {
  __shared__ __attribute__((aligned(16))) char smem_raw[L_END];
  LAS char* sm = (LAS char*)smem_raw;
  const unsigned tid = threadIdx.x, lane = tid & 63;
  const unsigned w = __builtin_amdgcn_readfirstlane(tid >> 6);
  const unsigned tg = w & 3, fg = w >> 2;
  const unsigned b = blockIdx.x, xcd = b & 7, bi = b >> 3;
  const unsigned r = (xcd & 1) * 8 + (bi & 7), c = (xcd >> 1) * 4 + (bi >> 3);
  const unsigned l31 = lane & 31, h = lane >> 5, blk = (lane >> 4) & 1, q = (lane & 15) >> 2, p = lane & 3;
  const bool kvwg = (c == 3) || (c == 7);
  STAMP(0); STAMPC(12);

  const u32x4 rs_ws = mkrs(P.ws, WS_END);
  const u32x4 rs_x = mkrs((const char*)P.x + r * 147456, 147456);
  const u32x4 rs_w1 = mkrs(P.w1, 576 * 9216);
  const u32x4 rs_wq = mkrs(P.wq, 576 * 1152);

  unsigned epoch, gw0, gw1 = 0, kc0 = 0, kc1 = 0;
  u32x4 ka0, kb0, ka1, kb1;
  aload1_sc1(epoch, rs_ws, 0u * lane, WS_EPOCH);
  {
    const u32x4 rs_gw = mkrs(P.gw, 1152);
    const u32x4 rs_kv = mkrs(c == 7 ? P.wv : P.wk, 576 * 36);
    aload1(gw0, rs_gw, (tid >> 1) * 4, 0);
    if (w == 0) aload1(gw1, rs_gw, (tid >> 1) * 4, 1024);
    if (kvwg) {
      aload4(ka0, rs_kv, tid * 36, 0); aload4(kb0, rs_kv, tid * 36, 16); aload1(kc0, rs_kv, tid * 36, 32);
      if (w == 0) { aload4(ka1, rs_kv, tid * 36, 512 * 36); aload4(kb1, rs_kv, tid * 36, 512 * 36 + 16); aload1(kc1, rs_kv, tid * 36, 512 * 36 + 32); }
    }
    const bool isb2 = (w == 1) || (w == 2);
    const u32x4 rs_b = mkrs(isb2 ? P.b2 : P.b1, isb2 ? 1152 : 9216);
    glds(rs_b, lane * 16 + (isb2 ? (w - 1) * 1024 : c * 576), 0, isb2 ? L_B2 + (w - 1) * 1024 : L_B1);
  }
  const unsigned swz4 = ((lane & 7) ^ (((lane >> 4) & 1) << 2)) << 4;
  const unsigned vx0 = (16 * w + (lane >> 3)) * 1152 + (((lane & 7) ^ (lane >> 4)) << 4);
  const unsigned vx1 = (16 * w + 8 + (lane >> 3)) * 1152 + (((lane & 7) ^ (4 + (lane >> 4))) << 4);
  const unsigned vw1 = (8 * w + (lane >> 3)) * 9216 + swz4;
  const unsigned vw2 = (8 * w + (lane >> 3)) * 1152 + swz4;
  const unsigned w1col = (288 * c + (c & 1) * 32) * 2;
  auto issue_p0 = [&](unsigned G) {
    const unsigned slot = (G % 3) * 24576;
    glds(rs_x, vx0, G * 128, slot + 8192 + w * 2048);
    glds(rs_x, vx1, G * 128, slot + 8192 + w * 2048 + 1024);
    glds(rs_w1, vw1, w1col + G * (64 * 9216), slot + w * 1024);
    glds_wq(rs_wq, vw2, ((72 * c) & ~15u) + G * (64 * 1152), L_H + G * 8192 + w * 1024);
  };
  auto issue_slab = [&](unsigned j, unsigned u) {
    if (ABL & 1) return;
    const unsigned so = w1col + 128 * j + u * (192 * 9216);
    glds(rs_w1, vw1, so, u * 24576 + w * 1024);
    glds(rs_w1, vw1, so + 64 * 9216, u * 24576 + 8192 + w * 1024);
    glds(rs_w1, vw1, so + 128 * 9216, u * 24576 + 16384 + w * 1024);
  };
  issue_p0(0); issue_p0(1);
  wait_vm<9>();
  TIE(epoch); TIE(gw0); TIE(gw1); TIE(kc0); TIE(kc1); TIE(ka0); TIE(kb0); TIE(ka1); TIE(kb1);
  const unsigned tag = __builtin_amdgcn_readfirstlane(epoch) + 1;
  {
    const unsigned sx = ((tid >> 1) & 1) << 6;
    const unsigned ra = L_H + tid * 128;
    const unsigned sh = (tid & 1) * 16;
    u32x2 o0 = {kc0, 0x3f80u | ((gw0 >> sh) << 16)};
    lds_st<u32x2>(sm, ra + (112 ^ sx), o0);
    if (kvwg) { lds_st<u32x4>(sm, ra + (80 ^ sx), ka0); lds_st<u32x4>(sm, ra + (96 ^ sx), kb0); }
    if (w == 0) {
      u32x2 o1 = {kc1, 0x3f80u | ((gw1 >> sh) << 16)};
      lds_st<u32x2>(sm, ra + 65536 + (112 ^ sx), o1);
      if (kvwg) { lds_st<u32x4>(sm, ra + 65536 + (80 ^ sx), ka1); lds_st<u32x4>(sm, ra + 65536 + (96 ^ sx), kb1); }
    }
  }

  const unsigned rot = tg + 2 * fg;
  unsigned xa[4], wa[4], qa[4];
  {
    const unsigned p8 = 4 * blk + p;
    const unsigned bo = p8 < 5 ? ((72 * c) & 15) + 32 * fg + 8 * p8 : (fg == 0 ? 40 + 8 * p8 : (p8 == 5 ? 104 : 112));
    const unsigned sq = (q >> 1) << 2;
#pragma unroll
    for (int e = 0; e < 4; ++e) {
      const unsigned sg = (e + rot) & 3;
      xa[e] = 8192 + (32 * tg + l31) * 128 + (((2 * sg + h) ^ ((l31 >> 1) & 7)) << 4);
      wa[e] = (8 * h + q) * 128 + (((4 * fg + 2 * blk + (p >> 1)) ^ sq) << 4) + 8 * (p & 1) + sg * 2048;
      qa[e] = L_H + (8 * h + q) * 128 + (((bo >> 4) ^ sq) << 4) + (bo & 8) + sg * 2048;
    }
  }
  bf16x8 xf[36], wf[3], qf[3];
  f32x16 acc = zero16(), accq = zero16();
  f32x4 csa = {0.f, 0.f, 0.f, 0.f};
  float ss0 = 0.f, ss1 = 0.f;
  const unsigned selu = ((lane & 15) == blk) ? 0x3f803f80u : 0u;
  const bf16x8 sel = asfrag(u32x4{selu, selu, selu, selu});
  const unsigned csw = L_CS + (fg * 4 + tg) * 128 + lane * 4;
  const unsigned csr = L_CS + fg * 512 + l31 * 4;
  float cs0, cs1, cs2, cs3;
  auto p0_read = [&]<int S>() {
    constexpr unsigned G = S >> 2, slot = (G % 3) * 24576;
    xf[S] = ldfrag(sm, xa[S & 3] + slot);
    wf[S % 3] = trfrag(sm, wa[S & 3] + slot, wa[S & 3] + slot + 512);
    if constexpr (G < 8) qf[S % 3] = trfrag(sm, qa[S & 3] + G * 8192, qa[S & 3] + G * 8192 + 512);
    else { const unsigned a = add64k(qa[S & 3]); qf[S % 3] = trfrag(sm, a, a + 512); }
  };
  auto slab_read = [&]<int S>() {
    constexpr unsigned o = (S / 12) * 24576 + ((S % 12) >> 2) * 8192;
    if constexpr (o < 65536) wf[S % 3] = trfrag(sm, wa[S & 3] + o, wa[S & 3] + o + 512);
    else { const unsigned a = add64k(wa[S & 3]); wf[S % 3] = trfrag(sm, a, a + 512); }
  };
  auto cs_write = [&]() { if (lane < 16) { lds_st<float>(sm, csw, csa[0]); lds_st<float>(sm, csw + 64, csa[1]); } };
  auto cs_read = [&]() { cs0 = lds_ld<float>(sm, csr); cs1 = lds_ld<float>(sm, csr + 128); cs2 = lds_ld<float>(sm, csr + 256); cs3 = lds_ld<float>(sm, csr + 384); };

  sync_vm<4>();
  issue_p0(2);
  STAMP(1);
  p0_read.template operator()<0>(); p0_read.template operator()<1>();
  Unroll<0, 36>::run([&]<int S>() {
    SB();
    if constexpr ((S & 3) == 2) {
      constexpr int G = (S >> 2) + 1;
      if constexpr (G == 9) cs_write();
      sync_vm<(G <= 7) ? 4 : 3>();
      if constexpr (G + 2 <= 8) issue_p0(G + 2); else issue_slab(1, G + 2 - 9);
      if constexpr (G == 9) cs_read();
    }
    if constexpr (S + 2 < 36) p0_read.template operator()<S + 2>(); else slab_read.template operator()<S + 2 - 36>();
    SB();
    acc = mfma32(wf[S % 3], xf[S], acc);
    accq = mfma32(qf[S % 3], xf[S], accq);
    if constexpr ((S & 3) == 0) csa = mfma16(sel, wf[S % 3], csa);
    if constexpr ((S & 3) < 2) {
      const u32x4 u = __builtin_bit_cast(u32x4, xf[S]);
#pragma unroll
      for (int i = 0; i < 4; ++i) { float a = bflo(u[i]), bq = bfhi(u[i]); ss0 = __builtin_fmaf(a, a, ss0); ss1 = __builtin_fmaf(bq, bq, ss1); }
      TIE(ss0); TIE(ss1);
    }
  });
  SB();
  STAMP(2);
  const unsigned tok = 32 * tg + l31;
  {
    const float qs = 0.34004497f;
    const unsigned qb = L_QR + fg * 5120 + tok * 40 + 8 * h;
    lds_st<u32x2>(sm, qb, u32x2{cvtpk(accq[0] * qs, accq[1] * qs), cvtpk(accq[2] * qs, accq[3] * qs)});
    lds_st<u32x2>(sm, qb + 16, u32x2{cvtpk(accq[4] * qs, accq[5] * qs), cvtpk(accq[6] * qs, accq[7] * qs)});
    float ss = ss0 + ss1; ss += swap32(ss);
    if (h == 0) {
      lds_st<u32x2>(sm, qb + 32, u32x2{cvtpk(accq[8] * qs, accq[9] * qs), cvtpk(accq[10] * qs, accq[11] * qs)});
      lds_st<float>(sm, L_SSQ + fg * 512 + tok * 4, ss);
      if (fg == 1) { lds_st<float>(sm, L_SUM + tok * 4, accq[14]); lds_st<float>(sm, L_GL + tok * 4, accq[15]); }
    }
    if (kvwg) {
      const unsigned kb = L_KVST + tok * 40;
      if (h == 1) lds_st<u32x2>(sm, kb + fg * 24, u32x2{cvtpk(accq[8], accq[9]), cvtpk(accq[10], accq[11])});
      if (fg == 0 || h == 0) lds_st<u32x2>(sm, kb + (fg ? 32 : 8 + 8 * h), u32x2{cvtpk(accq[12], accq[13]), cvtpk(accq[14], accq[15])});
    }
  }
  sync_lds();
  float rho, sig, gate;
  bf16x8 bext;
  Gelu gl;
  {
    const float s2 = lds_ld<float>(sm, L_SSQ + tok * 4) + lds_ld<float>(sm, L_SSQ + 512 + tok * 4);
    const float mu = lds_ld<float>(sm, L_SUM + tok * 4) * (1.f / 576.f);
    const float lg = lds_ld<float>(sm, L_GL + tok * 4);
    const float var = fmaxf(s2 * (1.f / 576.f) - mu * mu, 0.f);
    sig = __builtin_sqrtf(var + 1e-5f);
    rho = 1.f / sig;
    gate = 1.f / (1.f + __expf(-lg));
    const float mh = rbf(-mu), ml = -mu - mh, sh = rbf(sig), sm1 = rbf(sig - sh), sl = sig - sh - sm1;
    u32x4 be = {cvtpk(mh, ml), cvtpk(mh, sh), cvtpk(sm1, sl), 0u};
    if (h) be = u32x4{0u, 0u, 0u, 0u};
    bext = asfrag(be);
    gl.k1 = -2.302208198f * rho; gl.k2 = -0.102943240f * rho * rho * rho;
  }
  if (kvwg && fg == 0) {
    const unsigned T = 4 * r + tg;
    if (c == 3) {
      const unsigned kb = L_KVST + tok * 40;
      const u32x2 a0 = lds_ld<u32x2>(sm, kb + 16 * h), a1 = lds_ld<u32x2>(sm, kb + 16 * h + 8);
      const unsigned e2 = lds_ld<unsigned>(sm, kb + 32);
      float n2 = h ? 0.f : bflo(e2) * bflo(e2) + bfhi(e2) * bfhi(e2);
      n2 += bflo(a0[0]) * bflo(a0[0]) + bfhi(a0[0]) * bfhi(a0[0]) + bflo(a0[1]) * bflo(a0[1]) + bfhi(a0[1]) * bfhi(a0[1]);
      n2 += bflo(a1[0]) * bflo(a1[0]) + bfhi(a1[0]) * bfhi(a1[0]) + bflo(a1[1]) * bflo(a1[1]) + bfhi(a1[1]) * bfhi(a1[1]);
      n2 += swap32(n2);
#pragma unroll
      for (int m = 1; m < 32; m <<= 1) n2 = fmaxf(n2, __shfl_xor(n2, m));
      store4_sc1(u32x4{a0[0], a0[1], a1[0], a1[1]}, rs_ws, lane * 16, WS_KV + T * 3072);
      if (h == 0) store1_sc1(e2, rs_ws, lane * 4, WS_KV + T * 3072 + 1024 + 384);
      if (lane == 0) store1_sc1(__float_as_uint(n2), rs_ws, lane, WS_KNORM + T * 4);
    } else {
      const unsigned dcl = l31 < 18 ? l31 : 0;
#pragma unroll
      for (int u = 0; u < 2; ++u) {
        u32x4 v;
#pragma unroll
        for (int e = 0; e < 4; ++e) {
          const unsigned a = L_KVST + (32 * tg + 16 * u + 8 * h + 2 * e) * 40 + dcl * 2;
          const unsigned lo = lds_ld<unsigned short>(sm, a), hi = lds_ld<unsigned short>(sm, a + 40);
          v[e] = l31 < 18 ? (lo | (hi << 16)) : (l31 == 18 ? 0x3f803f80u : 0u);
        }
        if (u == 1 || lane < 24 || lane >= 32) store4_sc1(v, rs_ws, lane * 16, WS_KV + T * 3072 + 1024 + u * 1024);
      }
    }
  }
  STAMP(3);

  const unsigned hbase = L_H + tg * 18432 + lane * 16;
  const unsigned b1a = L_B1 + l31 * 2;
  const unsigned vhs = (16 * w + (lane >> 2)) * 9216 + (lane & 3) * 16;
  const unsigned hcol = (288 * c + ((c & 1) ? 0 : 256)) * 2;
  const unsigned hsb = (8 * h + q) * 64 + 32 * blk + 8 * p;
  const u32x4 rs_w2 = mkrs((const char*)P.w2 + c * (288 * 1152), 288 * 1152);
  auto issue_w2 = [&](unsigned j, unsigned slot) {
    if (ABL & 2) return;
#pragma unroll
    for (unsigned m = 0; m < 4; ++m) glds(rs_w2, vw2, 128 * j + m * (64 * 1152), slot + m * 8192 + w * 1024);
    glds_half(rs_w2, vw2, 128 * j + (256 - 4 * w) * 1152, slot + 32768 + w * 512);
  };
  f32x16 pacc;
  unsigned pslot = (c & 1) + fg;
#pragma unroll 1
  for (unsigned j = 1; j <= 3; ++j) {
    pacc = mfma32(make_aext((cs0 + cs1) + (cs2 + cs3), lds_ld<unsigned short>(sm, b1a + pslot * 64)), bext, acc);
    const unsigned haddr = hbase + pslot * 2048;
    const unsigned n0 = j < 3 ? wa[0] : hsb + (rot & 3) * 1024, n1 = j < 3 ? wa[1] : hsb + ((rot + 1) & 3) * 1024;
    const unsigned nt = j < 3 ? 512 : 256;
    Unroll<0, 36>::run([&]<int S>() {
      SB();
      if constexpr (S % 12 == 10) {
        if constexpr (S == 34) cs_write();
        if (S < 34 || j < 3) sync_vm<3>(); else sync_vm<2>();
        if constexpr (S == 22) if (j == 1 && kvwg && w == 0) set_flag(rs_ws, WS_KVFLAG + (r * 2 + (c >> 2)) * 4, tag);
        if (j < 3) issue_slab(j + 1, S / 12);
        else if constexpr (S == 10) {
#pragma unroll
          for (unsigned m = 0; m < 3; ++m) glds(rs_w1, vhs, hcol + m * (128 * 9216), (w + 8 * m) * 1024);
        } else if constexpr (S == 22) {
          glds(rs_w1, vhs, hcol + 384 * 9216, (24 + w) * 1024);
          glds(rs_w1, vhs, hcol + (512 - 16 * (w & 4)) * 9216, (32 + (w & 3)) * 1024);
        } else issue_w2(0, 36864);
        if constexpr (S == 34) cs_read();
      }
      if constexpr (S + 2 < 36) slab_read.template operator()<S + 2>();
      else if constexpr (S == 34) wf[0] = trfrag(sm, n0, n0 + nt);
      else wf[1] = trfrag(sm, n1, n1 + nt);
      SB();
      if constexpr (S == 0) acc = mfma32(wf[0], xf[0], zero16()); else acc = mfma32(wf[S % 3], xf[S], acc);
      if constexpr (S == 0) csa = mfma16(sel, wf[0], f32x4{0.f, 0.f, 0.f, 0.f});
      else if constexpr ((S & 3) == 0) csa = mfma16(sel, wf[S % 3], csa);
      gelu_step<S>(gl, pacc, sm, haddr);
    });
    pslot += 2;
  }
  SB();
  STAMP(4);
  {
    pacc = mfma32(make_aext((cs0 + cs1) + (cs2 + cs3), lds_ld<unsigned short>(sm, b1a + pslot * 64)), bext, acc);
    const unsigned haddr = hbase + pslot * 2048;
    if (fg == 0) {
      unsigned ha[4];
#pragma unroll
      for (int e = 0; e < 4; ++e) ha[e] = hsb + ((e + rot) & 3) * 1024;
      Unroll<0, 36>::run([&]<int S>() {
        SB();
        if constexpr (S == 22) sync_vm<5>();
        if constexpr (S == 34) { cs_write(); sync_lds(); cs_read(); }
        if constexpr (S + 2 < 36) wf[(S + 2) % 3] = trfrag(sm, ha[(S + 2) & 3] + ((S + 2) >> 2) * 4096, ha[(S + 2) & 3] + ((S + 2) >> 2) * 4096 + 256);
        SB();
        if constexpr (S == 0) acc = mfma32(wf[0], xf[0], zero16()); else acc = mfma32(wf[S % 3], xf[S], acc);
        if constexpr (S == 0) csa = mfma16(sel, wf[0], f32x4{0.f, 0.f, 0.f, 0.f});
        else if constexpr ((S & 3) == 0) csa = mfma16(sel, wf[S % 3], csa);
        gelu_step<S>(gl, pacc, sm, haddr);
      });
      SB();
      const unsigned lslot = (c & 1) ? 0 : 8;
      pacc = mfma32(make_aext((cs0 + cs1) + (cs2 + cs3), lds_ld<unsigned short>(sm, b1a + lslot * 64)), bext, acc);
      Unroll<0, 36>::run([&]<int S>() { gelu_step<S>(gl, pacc, sm, hbase + lslot * 2048); });
    } else {
      Unroll<0, 36>::run([&]<int S>() { gelu_step<S>(gl, pacc, sm, haddr); });
      sync_vm<5>();
      sync_lds();
    }
  }
  sync_lds();
  issue_w2(1, 0);
  STAMP(5);

  bf16x8 hf[18];
#pragma unroll
  for (int u = 0; u < 18; ++u) hf[u] = ldfrag(sm, hbase + u * 1024);
  sync_vm<5>();
  issue_w2(2, 73728);
  const unsigned w2a = (4 * h + q) * 128 + (((4 * fg + 2 * blk + (p >> 1)) ^ ((q >> 1) << 2)) << 4) + 8 * (p & 1);
  const float osc = rho * (1.f - gate);
  const unsigned pso = WS_PART + (4 * r + tg) * (18 * 32768) + fg * 32768 + c * 2048;
  unsigned kvf, knorm;
  const unsigned vkv0 = w * 1024 + lane * 16 + (w >= 6 ? 32 * 3072 - 6144 : 0);
  const unsigned vkv1 = 8192 + w * 512 + lane * 16 + 32 * 3072 - 6144;
  auto issue_kv = [&](unsigned st, unsigned slot) {
    glds_sc1(rs_ws, vkv0, WS_KV + st * 6144, L_RING + slot + w * 1024);
    glds_half_sc1(rs_ws, vkv1, WS_KV + st * 6144, L_RING + slot + 8192 + w * 512);
  };
  unsigned pk0, pk1, pk2;
  auto part_epi = [&]<int E>(unsigned jprev) {
    const unsigned pk = cvtpk(pacc[2 * E] * osc, pacc[2 * E + 1] * osc);
    if constexpr ((E & 3) == 0) pk0 = pk;
    if constexpr ((E & 3) == 1) pk1 = pk;
    if constexpr ((E & 3) == 2) pk2 = pk;
    if constexpr ((E & 3) == 3 && !(ABL & 4)) store4_sc1(u32x4{pk0, pk1, pk2, pk}, rs_ws, lane * 16, pso + jprev * 65536 + (E >> 2) * 1024);
  };
  {
    unsigned cur = 36864, nxt = 0, aft = 73728;
    wf[0] = trfrag(sm, w2a + cur, w2a + cur + 1024);
    wf[1] = trfrag(sm, w2a + cur + 2048, w2a + cur + 2048 + 1024);
#pragma unroll 1
    for (unsigned j = 0; j < 9; ++j) {
      const unsigned ca = w2a + cur, na = w2a + nxt;
      f32x16 ini = zero16();
      if (2 * j + fg == c || 2 * j + fg == c + 16) {
#pragma unroll
        for (int g4 = 0; g4 < 4; ++g4) {
          const u32x2 bv = lds_ld<u32x2>(sm, L_B2 + (2 * j + fg) * 64 + g4 * 16 + h * 8);
          ini[4 * g4] = bflo(bv[0]) * sig; ini[4 * g4 + 1] = bfhi(bv[0]) * sig;
          ini[4 * g4 + 2] = bflo(bv[1]) * sig; ini[4 * g4 + 3] = bfhi(bv[1]) * sig;
        }
      }
      Unroll<0, 18>::run([&]<int U>() {
        SB();
        if constexpr (U == 16) {
          if (j < 8) {
            if (j == 0) wait_vm<5>(); else if (j == 1 || j == 6) wait_vm<7>(); else wait_vm<9>();
            sync_lds();
            if (j == 5) aload1_sc1(kvf, rs_ws, l31 * 4, WS_KVFLAG);
            if (j <= 5) issue_w2(j + 3, cur);
            if (j == 6) {
              TIE(kvf);
              if (!__all(kvf == tag)) spin_flags(P.ws, WS_KVFLAG + l31 * 4, true, tag);
              aload1_sc1(knorm, rs_ws, lane * 4, WS_KNORM);
              issue_kv(0, 0); issue_kv(1, 12288);
            }
          }
        }
        if constexpr (U + 2 < 18) wf[(U + 2) % 3] = trfrag(sm, ca + (U + 2) * 2048, ca + (U + 2) * 2048 + 1024);
        else wf[(U + 2) % 3] = trfrag(sm, na + (U - 16) * 2048, na + (U - 16) * 2048 + 1024);
        SB();
        if constexpr (U == 0) acc = mfma32(wf[0], hf[0], ini); else acc = mfma32(wf[U % 3], hf[U], acc);
        if constexpr (U >= 2 && U < 10) { if (j > 0) part_epi.template operator()<U - 2>(j - 1); }
      });
      pacc = acc;
      const unsigned t = cur; cur = nxt; nxt = aft; aft = t;
    }
    SB();
    Unroll<0, 8>::run([&]<int E>() { part_epi.template operator()<E>(8); });
  }
  STAMP(6);

  const unsigned ti = bi < 24 ? bi : bi - 24;
  const unsigned T2 = xcd * 4 + ti / 6, nb = ti % 6, tg2 = w & 1, kq = w >> 1;
  bf16x8 qA[2], qB[2];
  wait_vm<10>();
  TIE(knorm);
  {
    float kmax = __uint_as_float(knorm);
#pragma unroll
    for (int m = 1; m < 64; m <<= 1) kmax = fmaxf(kmax, __shfl_xor(kmax, m));
    unsigned e2[2]; float M[2];
#pragma unroll
    for (int hd = 0; hd < 2; ++hd) {
      const unsigned qb = L_QR + hd * 5120 + tok * 40 + 4 * hd;
      u32x4 v;
#pragma unroll
      for (int i = 0; i < 4; ++i) v[i] = lds_ld<unsigned>(sm, qb + 16 * h + 4 * i);
      e2[hd] = lds_ld<unsigned>(sm, qb + 32);
      qA[hd] = asfrag(v);
      float n2 = h ? 0.f : bflo(e2[hd]) * bflo(e2[hd]) + bfhi(e2[hd]) * bfhi(e2[hd]);
#pragma unroll
      for (int i = 0; i < 4; ++i) n2 += bflo(v[i]) * bflo(v[i]) + bfhi(v[i]) * bfhi(v[i]);
      n2 += swap32(n2);
      M[hd] = __builtin_sqrtf(n2 * kmax) * 1.01f;
    }
    if (!__all(M[0] <= 50.f && M[1] <= 50.f)) {
      const bf16x8 z0 = asfrag(u32x4{h ? 0u : e2[0], 0u, 0u, 0u}), z1 = asfrag(u32x4{h ? 0u : e2[1], 0u, 0u, 0u});
      float m0 = -3.0e38f, m1 = -3.0e38f;
#pragma unroll 1
      for (unsigned T = 0; T < 64; ++T) {
        u32x4 kau; unsigned kbu;
        aload4_sc1(kau, rs_ws, lane * 16, WS_KV + T * 3072);
        aload1_sc1(kbu, rs_ws, l31 * 4, WS_KV + T * 3072 + 1408);
        wait_vm<0>();
        TIE(kau); TIE(kbu);
        const bf16x8 ka = asfrag(kau), kb = asfrag(u32x4{kbu, 0u, 0u, 0u});
        const f32x16 s0 = mfma32(kb, z0, mfma32(ka, qA[0], zero16())), s1 = mfma32(kb, z1, mfma32(ka, qA[1], zero16()));
#pragma unroll
        for (int i = 0; i < 16; ++i) { m0 = fmaxf(m0, s0[i]); m1 = fmaxf(m1, s1[i]); }
      }
      M[0] = fmaxf(m0, swap32(m0)); M[1] = fmaxf(m1, swap32(m1));
    }
#pragma unroll
    for (int hd = 0; hd < 2; ++hd) {
      const float a = rbf(-M[hd]), bq = rbf(-M[hd] - a), cq = -M[hd] - a - bq;
      u32x4 v = {e2[hd], cvtpk(a, bq), cvtpk(cq, 0.f), 0u};
      if (h) v = u32x4{0u, 0u, 0u, 0u};
      qB[hd] = asfrag(v);
    }
  }
  const u32x4 rs_wo = mkrs(P.wo, 576 * 1152);
  const unsigned pfo = WS_PFLAG + ((T2 >> 1) * 32 + 4 * kq + (lane & 3)) * 4;
  const unsigned plo = WS_PART + ((2 * T2 + tg2) * 18 + 3 * nb) * 32768 + kq * 8192;
  bf16x8 idA[2];
#pragma unroll
  for (int s = 0; s < 2; ++s) {
    u32x4 v;
#pragma unroll
    for (int i = 0; i < 4; ++i) {
      const unsigned row = 16 * s + 8 * (i >> 1) + 4 * h + 2 * (i & 1);
      v[i] = l31 == row ? 0x3f80u : (l31 == row + 1 ? 0x3f800000u : 0u);
    }
    idA[s] = asfrag(v);
  }
  f32x16 O0 = zero16(), O1 = zero16(), pm0 = zero16(), pm1 = zero16(), pm2 = zero16(), Sa, Sb;
  u32x4 pf0 = {0u, 0u, 0u, 0u}, pf1 = {0u, 0u, 0u, 0u}, kB0 = {0u, 0x3f803f80u, 0x3f80u, 0u}, kB1 = kB0;
  bf16x8 kA0, kA1, v00, v01, v10, v11;
  unsigned pflg, woi = w * 64 + lane;
  const unsigned kva = L_RING + fg * 6144 + lane * 16, kba = L_RING + fg * 6144 + 1408 + l31 * 4;
  auto issue_wo = [&](unsigned n) {
    if (ABL & 16) return;
    const unsigned idx = n * 64 + lane, row = idx / 12;
    glds(rs_wo, row * 1152 + (idx - row * 12) * 16, nb * 192, n * 1024);
  };
  auto expo = [&](f32x16& S, int a, int n) {
#pragma unroll
    for (int i = 0; i < 8; ++i) if (i < n && !(ABL & 64)) S[a + i] = __builtin_amdgcn_exp2f(S[a + i]);
  };
  auto pv = [&](f32x16& O, const bf16x8& va, const f32x16& S, int u) {
    f32x8 x;
#pragma unroll
    for (int i = 0; i < 8; ++i) x[i] = S[8 * u + i];
    if (!(ABL & 128)) O = __builtin_amdgcn_smfmac_f32_32x32x32_bf16(va, __builtin_bit_cast(bf16x16, x), O, 0xDDDD, 0, 0);
  };
  auto unit = [&](f32x16& C, f32x16& O, const bf16x8& va, const bf16x8& vb, f32x16& N, const bf16x8& ka, const u32x4& kb, int hd) {
    expo(C, 0, 4); SB();
    N = mfma32(ka, qA[hd], zero16()); SB();
    expo(C, 4, 4); SB();
    pv(O, va, C, 0); SB();
    expo(C, 8, 4); SB();
    N = mfma32(asfrag(kb), qB[hd], N); SB();
    expo(C, 12, 4); SB();
    pv(O, vb, C, 1); SB();
  };
  sync_vm<8>();
  issue_wo(w); issue_kv(2, 24576);
  kA0 = ldfrag(sm, kva); kB0[0] = lds_ld<unsigned>(sm, kba); v00 = ldfrag(sm, kva + 1024); v01 = ldfrag(sm, kva + 2048);
  Sa = mfma32(asfrag(kB0), qB[0], mfma32(kA0, qA[0], zero16()));
  STAMP(7); STAMPC(14);
  unsigned so = 0;
#pragma unroll 1
  for (unsigned st = 0; st < 16; ++st) {
    SB();
    kA1 = ldfrag(sm, kva + so + 3072); kB1[0] = lds_ld<unsigned>(sm, kba + so + 3072);
    v10 = ldfrag(sm, kva + so + 4096); v11 = ldfrag(sm, kva + so + 5120);
    SB();
    unit(Sa, O0, v00, v01, Sb, kA0, kB0, 1);
    unit(Sb, O1, v00, v01, Sa, kA1, kB1, 0);
    const unsigned sn = st + 1;
    const unsigned fr = so;
    so = so == 24576 ? 0 : so + 12288;
    if (sn < 16) {
      if (!(ABL & 32)) {
      if (sn <= 14) wait_vm<2>(); else wait_vm<0>();
      sync_lds(); }
      TIE(pf0); TIE(pf1);
      if (sn >= 4) {
        if (sn < 8) { pm0 = mfma32(idA[0], asfrag(pf0), pm0); pm0 = mfma32(idA[1], asfrag(pf1), pm0); }
        else if (sn < 12) { pm1 = mfma32(idA[0], asfrag(pf0), pm1); pm1 = mfma32(idA[1], asfrag(pf1), pm1); }
        else { pm2 = mfma32(idA[0], asfrag(pf0), pm2); pm2 = mfma32(idA[1], asfrag(pf1), pm2); }
      }
      if (sn == 1 && w == 0) set_flag(rs_ws, WS_PFLAG + (r * 32 + c) * 4, tag);
      if (sn == 2) aload1_sc1(pflg, rs_ws, pfo, 0);
      if (sn == 3) {
        TIE(pflg);
        if (!(ABL & 256)) if (!__all(pflg == tag)) spin_flags(P.ws, pfo, true, tag);
      }
      SB();
      if (sn >= 3 && sn <= 14 && !(ABL & 8)) {
        const unsigned i = sn - 3, o = plo + (i >> 2) * 32768 + (i & 3) * 2048;
        aload4_sc1(pf0, rs_ws, lane * 16, o); aload4_sc1(pf1, rs_ws, lane * 16, o + 1024);
      }
      if (sn <= 13) { const unsigned n = sn * 8 + w; issue_wo(n < 108 ? n : n - 4); issue_kv(sn + 2, fr); }
      kA0 = ldfrag(sm, kva + so); kB0[0] = lds_ld<unsigned>(sm, kba + so);
      v00 = ldfrag(sm, kva + so + 1024); v01 = ldfrag(sm, kva + so + 2048);
    }
    SB();
    unit(Sa, O0, v10, v11, Sb, kA1, kB1, 1);
    unit(Sb, O1, v10, v11, Sa, kA0, kB0, 0);
  }
  STAMP(8); STAMPC(15);
  {
    const unsigned xw = L_RING + w * 2816 + lane * 4, xr = L_RING + (w ^ 4) * 2816 + lane * 4;
    sync_lds();
#pragma unroll
    for (int i = 0; i < 11; ++i) lds_st<float>(sm, xw + i * 256, fg ? O0[i] : O1[i]);
    sync_lds();
    float o[11];
#pragma unroll
    for (int i = 0; i < 11; ++i) o[i] = (fg ? O1[i] : O0[i]) + lds_ld<float>(sm, xr + i * 256);
    const float lsw = swap32(o[10]);
    const float sc = gate / (h ? lsw : o[10]);
    const unsigned T = 4 * r + tg, hd = 2 * c + fg;
    store4_sc1(u32x4{cvtpk(o[0] * sc, o[1] * sc), cvtpk(o[2] * sc, o[3] * sc), cvtpk(o[4] * sc, o[5] * sc), cvtpk(o[6] * sc, o[7] * sc)},
               rs_ws, lane * 16, WS_O + (T * 36 + hd) * 1024);
    if (h == 0) store1_sc1(cvtpk(o[8] * sc, o[9] * sc), rs_ws, l31 * 16, WS_O + (T * 36 + 32 + (hd >> 3)) * 1024 + ((hd >> 2) & 1) * 512 + (hd & 3) * 4);
    sync_vm<0>();
    if (w == 0) set_flag(rs_ws, WS_OFLAG + (r * 32 + c) * 4, tag);
  }
  STAMP(9); STAMPC(13);
  if (bi >= 24) {
    if (b == 255 && w == 0) {
#pragma unroll
      for (int m = 0; m < 4; ++m) spin_flags(P.ws, WS_PFLAG + ((m * 4 + (lane >> 4)) * 32 + (lane & 15)) * 4, true, tag);
      set_flag(rs_ws, WS_EPOCH, tag);
    }
    return;
  }

  {
    bf16x8 wo[27];
    const unsigned wm = (144 * kq + 4 * h + q) * 192 + 32 * blk + 8 * p;
    const unsigned wr = (18 * (8 * kq + 4 * h + (q >> 1)) + 16 + (q & 1)) * 192 + 32 * blk + 8 * p;
#pragma unroll
    for (int a = 0; a < 3; ++a) {
#pragma unroll
      for (int i = 0; i < 8; ++i) wo[a * 9 + i] = trfrag(sm, wm + i * 3456 + a * 64, wm + i * 3456 + a * 64 + 1536);
      wo[a * 9 + 8] = trfrag(sm, wr + a * 64, wr + a * 64 + 6912);
    }
    spin_flags(P.ws, WS_OFLAG + ((T2 >> 1) * 32 + 4 * kq + (lane & 3)) * 4, true, tag);
    u32x4 of[9];
    const unsigned oo = WS_O + (2 * T2 + tg2) * 36864;
#pragma unroll
    for (int i = 0; i < 8; ++i) aload4_sc1(of[i], rs_ws, lane * 16, oo + (8 * kq + i) * 1024);
    aload4_sc1(of[8], rs_ws, lane * 16, oo + (32 + kq) * 1024);
    sync_lds();
    STAMP(10);
    wait_vm<0>();
#pragma unroll
    for (int i = 0; i < 9; ++i) {
      TIE(of[i]);
      pm0 = mfma32(wo[i], asfrag(of[i]), pm0); pm1 = mfma32(wo[9 + i], asfrag(of[i]), pm1); pm2 = mfma32(wo[18 + i], asfrag(of[i]), pm2);
    }
    const unsigned ra = kq * 25600 + (32 * tg2 + l31) * 400 + 16 * h;
#pragma unroll
    for (int g4 = 0; g4 < 4; ++g4) {
      lds_st<f32x4>(sm, ra + g4 * 32, f32x4{pm0[4 * g4], pm0[4 * g4 + 1], pm0[4 * g4 + 2], pm0[4 * g4 + 3]});
      lds_st<f32x4>(sm, ra + 128 + g4 * 32, f32x4{pm1[4 * g4], pm1[4 * g4 + 1], pm1[4 * g4 + 2], pm1[4 * g4 + 3]});
      lds_st<f32x4>(sm, ra + 256 + g4 * 32, f32x4{pm2[4 * g4], pm2[4 * g4 + 1], pm2[4 * g4 + 2], pm2[4 * g4 + 3]});
    }
    sync_lds();
#pragma unroll
    for (int m = 0; m < 3; ++m) {
      const unsigned idx = tid + m * 512, t = idx / 24, cg = idx - t * 24;
      const unsigned a = t * 400 + cg * 16;
      const f32x4 s = (lds_ld<f32x4>(sm, a) + lds_ld<f32x4>(sm, a + 25600)) + (lds_ld<f32x4>(sm, a + 51200) + lds_ld<f32x4>(sm, a + 76800));
      *(GAS u32x2*)((GAS char*)P.out + (64 * T2 + t) * 1152 + nb * 192 + cg * 8) = u32x2{cvtpk(s[0], s[1]), cvtpk(s[2], s[3])};
    }
  }
  STAMP(11);
}

extern "C" void kernel_launch(void* const* d_in, const int* in_sizes, int n_in, void* d_out, int out_size,
                              void* d_ws, size_t ws_size, hipStream_t stream) {
  Params P;
  P.x = d_in[0]; P.wq = d_in[1]; P.wk = d_in[2]; P.wv = d_in[3]; P.wo = d_in[4];
  P.w1 = d_in[6]; P.b1 = d_in[7]; P.w2 = d_in[8]; P.b2 = d_in[9]; P.gw = d_in[10];
  P.out = d_out; P.ws = d_ws;
  fused_kernel<<<256, 512, 0, stream>>>(P);
}
```

```cpp
#include <hip/hip_runtime.h>
#include <hip/hip_bf16.h>

typedef __bf16 bf16x8 __attribute__((ext_vector_type(8)));
typedef __bf16 bf16x16 __attribute__((ext_vector_type(16)));
typedef short s16x4 __attribute__((ext_vector_type(4)));
typedef float f32x4 __attribute__((ext_vector_type(4)));
typedef float f32x8 __attribute__((ext_vector_type(8)));
typedef float f32x16 __attribute__((ext_vector_type(16)));
typedef unsigned u32x2 __attribute__((ext_vector_type(2)));
typedef unsigned u32x4 __attribute__((ext_vector_type(4)));
#define LAS __attribute__((address_space(3)))
#define GAS __attribute__((address_space(1)))
#define DI __device__ __forceinline__
#define SB() __builtin_amdgcn_sched_barrier(0)

constexpr unsigned WS_EPOCH = 0;
constexpr unsigned WS_KVFLAG = 256;
constexpr unsigned WS_PFLAG = 1024;
constexpr unsigned WS_KNORM = 8192;
constexpr unsigned WS_STAMP = 16384;
constexpr unsigned WS_KV = 262144;
constexpr unsigned WS_O = 524288;
constexpr unsigned WS_PART = 4194304;
constexpr unsigned WS_END = WS_PART + 64u * 18u * 16u * 2048u;

constexpr unsigned L_H = 73728;
constexpr unsigned L_KVST = L_H + 61440;
constexpr unsigned L_RING = 110592;
constexpr unsigned L_M = 147456;
constexpr unsigned L_QR = L_M;
constexpr unsigned L_SSQ = L_M + 10240;
constexpr unsigned L_SUM = L_M + 11264;
constexpr unsigned L_GL = L_M + 11776;
constexpr unsigned L_CS = L_M + 12288;
constexpr unsigned L_B1 = L_M + 13312;
constexpr unsigned L_B2 = L_M + 14336;
constexpr unsigned L_END = 163840;

typedef unsigned long long u64x8 __attribute__((ext_vector_type(8)));
typedef unsigned long long u64x4 __attribute__((ext_vector_type(4)));
struct Params {
  const void* x; const void* wq; const void* wk; const void* wv; const void* wo;
  const void* w1; const void* b1; const void* w2; const void* b2; const void* gw;
  void* out; void* ws;
};

DI u32x4 mkrs(const void* p, unsigned bytes) {
  u32x4 r = {(unsigned)(size_t)p, (unsigned)((size_t)p >> 32) & 0xffffu, bytes, 0x20000u};
  return r;
}
DI void glds(u32x4 rs, unsigned voff, unsigned soff, unsigned ldsb) {
  asm volatile("s_mov_b32 m0, %3\n\ts_nop 0\n\tbuffer_load_dwordx4 %0, %1, %2 offen lds"
               :: "v"(voff), "s"(rs), "s"(soff), "s"(ldsb) : "memory", "m0");
}
DI void touch(u32x4 rs, unsigned voff, unsigned ldsb) {
  asm volatile("s_mov_b32 m0, %2\n\ts_nop 0\n\tbuffer_load_dword %0, %1, 0 offen lds" :: "v"(voff), "s"(rs), "s"(ldsb) : "memory", "m0");
}
DI void glds_sc1(u32x4 rs, unsigned voff, unsigned soff, unsigned ldsb) {
  asm volatile("s_mov_b32 m0, %3\n\ts_nop 0\n\tbuffer_load_dwordx4 %0, %1, %2 offen sc1 lds"
               :: "v"(voff), "s"(rs), "s"(soff), "s"(ldsb) : "memory", "m0");
}
DI void glds_half_sc1(u32x4 rs, unsigned voff, unsigned soff, unsigned ldsb) {
  asm volatile("s_mov_b32 m0, %3\n\ts_mov_b32 exec_hi, 0\n\tbuffer_load_dwordx4 %0, %1, %2 offen sc1 lds\n\ts_mov_b32 exec_hi, -1"
               :: "v"(voff), "s"(rs), "s"(soff), "s"(ldsb) : "memory", "m0");
}
DI void glds_half(u32x4 rs, unsigned voff, unsigned soff, unsigned ldsb) {
  asm volatile("s_mov_b32 m0, %3\n\ts_mov_b32 exec_hi, 0\n\tbuffer_load_dwordx4 %0, %1, %2 offen lds\n\ts_mov_b32 exec_hi, -1"
               :: "v"(voff), "s"(rs), "s"(soff), "s"(ldsb) : "memory", "m0");
}
DI void glds_wq(u32x4 rs, unsigned voff, unsigned soff, unsigned ldsb) {
  asm volatile("s_mov_b32 m0, %3\n\ts_mov_b32 exec_lo, 0xf1f11f1f\n\ts_mov_b32 exec_hi, 0xf1f11f1f\n\t"
               "buffer_load_dwordx4 %0, %1, %2 offen lds\n\ts_mov_b64 exec, -1"
               :: "v"(voff), "s"(rs), "s"(soff), "s"(ldsb) : "memory", "m0");
}
DI void aload4(u32x4& d, u32x4 rs, unsigned voff, unsigned soff) {
  asm volatile("buffer_load_dwordx4 %0, %1, %2, %3 offen" : "=&v"(d) : "v"(voff), "s"(rs), "s"(soff) : "memory");
}
DI void aload1(unsigned& d, u32x4 rs, unsigned voff, unsigned soff) {
  asm volatile("buffer_load_dword %0, %1, %2, %3 offen" : "=&v"(d) : "v"(voff), "s"(rs), "s"(soff) : "memory");
}
DI void aload1_sc1(unsigned& d, u32x4 rs, unsigned voff, unsigned soff) {
  asm volatile("buffer_load_dword %0, %1, %2, %3 offen sc1" : "=&v"(d) : "v"(voff), "s"(rs), "s"(soff) : "memory");
}
DI void aload4_sc1(u32x4& d, u32x4 rs, unsigned voff, unsigned soff) {
  asm volatile("buffer_load_dwordx4 %0, %1, %2, %3 offen sc1" : "=&v"(d) : "v"(voff), "s"(rs), "s"(soff) : "memory");
}
DI void aload2_sc1(u32x2& d, u32x4 rs, unsigned voff, unsigned soff) {
  asm volatile("buffer_load_dwordx2 %0, %1, %2, %3 offen sc1" : "=&v"(d) : "v"(voff), "s"(rs), "s"(soff) : "memory");
}
DI void store2_sc1(u32x2 d, u32x4 rs, unsigned voff, unsigned soff) {
  asm volatile("buffer_store_dwordx2 %0, %1, %2, %3 offen sc1" :: "v"(d), "v"(voff), "s"(rs), "s"(soff) : "memory");
}
DI void store4_sc1(u32x4 d, u32x4 rs, unsigned voff, unsigned soff) {
  asm volatile("buffer_store_dwordx4 %0, %1, %2, %3 offen sc1\n\ts_nop 1" :: "v"(d), "v"(voff), "s"(rs), "s"(soff) : "memory");
}
DI void store1_sc1(unsigned d, u32x4 rs, unsigned voff, unsigned soff) {
  asm volatile("buffer_store_dword %0, %1, %2, %3 offen sc1" :: "v"(d), "v"(voff), "s"(rs), "s"(soff) : "memory");
}
#define TIE(v) asm volatile("" : "+v"(v))
#define PAD(v, n) asm volatile("s_nop " #n : "+v"(v))
template <int N> DI void wait_vm() { asm volatile("s_waitcnt vmcnt(%0)" :: "n"(N) : "memory"); }
template <int N> DI void sync_vm() { asm volatile("s_waitcnt vmcnt(%0) lgkmcnt(0)\n\ts_barrier" :: "n"(N) : "memory"); }
DI void sync_lds() { asm volatile("s_waitcnt lgkmcnt(0)\n\ts_barrier" ::: "memory"); }

DI unsigned cvtpk(float a, float b) { unsigned r; asm("v_cvt_pk_bf16_f32 %0, %1, %2" : "=v"(r) : "v"(a), "v"(b)); return r; }
DI unsigned umax(unsigned a, unsigned b) { return a > b ? a : b; }
DI unsigned pkw(float a, float b) { const unsigned u = cvtpk(a, b); return u < 0xfffffffeu ? u : 0xfffffffeu; }
DI float bflo(unsigned u) { return __uint_as_float(u << 16); }
DI float bfhi(unsigned u) { return __uint_as_float(u & 0xffff0000u); }
DI float rbf(float a) { return bflo(cvtpk(a, a)); }
DI float dot2(unsigned u, float c) { asm("v_dot2c_f32_bf16 %0, %1, %1" : "+v"(c) : "v"(u)); return c; }
DI float swap32(float v) { return __shfl_xor(v, 32); }
DI float low32(float v) {
  return __uint_as_float(__builtin_amdgcn_permlane32_swap(__float_as_uint(v), __float_as_uint(v), false, false)[0]);
}
DI float sum32(float v) {
  const u32x2 t = __builtin_amdgcn_permlane32_swap(__float_as_uint(v), __float_as_uint(v), false, false);
  return __uint_as_float(t[0]) + __uint_as_float(t[1]);
}

DI bf16x8 trfrag(LAS char* sm, unsigned a0, unsigned a1) {
  s16x4 lo = __builtin_amdgcn_ds_read_tr16_b64_v4i16((LAS s16x4*)(sm + a0));
  s16x4 hi = __builtin_amdgcn_ds_read_tr16_b64_v4i16((LAS s16x4*)(sm + a1));
  u32x2 l = __builtin_bit_cast(u32x2, lo), h = __builtin_bit_cast(u32x2, hi);
  u32x4 r = {l[0], l[1], h[0], h[1]};
  return __builtin_bit_cast(bf16x8, r);
}
DI bf16x8 ldfrag(LAS char* sm, unsigned a) { return *(LAS bf16x8*)(sm + a); }
DI f32x16 mfma32(bf16x8 a, bf16x8 b, f32x16 c) { return __builtin_amdgcn_mfma_f32_32x32x16_bf16(a, b, c, 0, 0, 0); }
DI f32x4 mfma16(bf16x8 a, bf16x8 b, f32x4 c) { return __builtin_amdgcn_mfma_f32_16x16x32_bf16(a, b, c, 0, 0, 0); }

DI void spin_flags(const void* ws, unsigned off, bool pred, unsigned tag) {
  GAS unsigned* p = (GAS unsigned*)((GAS char*)ws + off);
#pragma unroll 1
  for (unsigned it = 0; it < (1u << 22); ++it) {
    unsigned v = pred ? __hip_atomic_load(p, __ATOMIC_RELAXED, __HIP_MEMORY_SCOPE_AGENT) : tag;
    if (__all(v == tag)) break;
    __builtin_amdgcn_s_sleep(1);
  }
}
DI void set_flag(u32x4 rs, unsigned off, unsigned tag) {
  unsigned z = 0, t = tag;
  asm volatile("s_mov_b64 exec, 1\n\tbuffer_store_dword %0, %1, %2, %3 offen sc1\n\ts_mov_b64 exec, -1"
               :: "v"(t), "v"(z), "s"(rs), "s"(off) : "memory");
}
#define STAMP2(i)
#define STAMP(i)
#define ABL 0
constexpr unsigned CODE_LEN = 51200;
#define G2PD 2
#define PRIO 4
#define OUTMOD ""
#define HOIST 1
#define FIRST0 0
#define WOT 0
#define HFP 3
#define WOPRE 11

template <class T> DI T lds_ld(LAS char* sm, unsigned a) { return *(LAS T*)(sm + a); }
template <class T> DI void lds_st(LAS char* sm, unsigned a, T v) { *(LAS T*)(sm + a) = v; }
DI bf16x8 asfrag(u32x4 v) { return __builtin_bit_cast(bf16x8, v); }
DI unsigned add64k(unsigned a) { unsigned r; asm volatile("v_add_u32 %0, 0x10000, %1" : "=v"(r) : "v"(a)); return r; }
DI f32x16 zero16() { f32x16 z; for (int i = 0; i < 16; ++i) z[i] = 0.f; return z; }

struct Gelu {
  float k1, k2, k3, t, ex, rc, y0;
  unsigned hp0, hp1, hp2;
};
template <int S> DI void gelu_step(Gelu& g, const f32x16& pa, LAS char* sm, unsigned haddr) {
  if constexpr (S >= 2 && S < 36) {
    constexpr int e = (S - 2) >> 1;
    if constexpr ((S & 1) == 0) {
      float d;
      if constexpr (e >= 1) d = __builtin_fmaf(g.ex, g.k3, g.k3);
      if constexpr (e < 16) g.t = __builtin_fmaf(g.k2, pa[e] * pa[e], g.k1);
      if constexpr (e >= 1) g.rc = __builtin_amdgcn_rcpf(d);
    } else {
      float z;
      if constexpr (e < 16) z = pa[e] * g.t;
      if constexpr (e >= 1) {
        constexpr int f = e - 1;
        const float y = pa[f] * g.rc;
        if constexpr ((f & 1) == 0) g.y0 = y;
        else {
          const unsigned pk = cvtpk(g.y0, y);
          if constexpr ((f & 7) == 1) g.hp0 = pk;
          if constexpr ((f & 7) == 3) g.hp1 = pk;
          if constexpr ((f & 7) == 5) g.hp2 = pk;
          if constexpr ((f & 7) == 7) { u32x4 v = {g.hp0, g.hp1, g.hp2, pk}; lds_st<u32x4>(sm, haddr + (f >> 3) * 1024, v); }
        }
      }
      if constexpr (e < 16) g.ex = __builtin_amdgcn_exp2f(z);
    }
  }
}
template <int S, int E> struct Unroll {
  template <class F> static DI void run(F&& f) { f.template operator()<S>(); Unroll<S + 1, E>::run(f); }
};
template <int E> struct Unroll<E, E> { template <class F> static DI void run(F&&) {} };

DI bf16x8 make_aext(float cs, unsigned b1u) {
  unsigned hh = cvtpk(cs, cs);
  float lo = cs - bflo(hh);
  unsigned bb = b1u | (b1u << 16);
  unsigned lb = (cvtpk(lo, lo) & 0xffffu) | (b1u << 16);
  u32x4 r = {hh, lb, bb, 0u};
  return asfrag(r);
}

__global__ __launch_bounds__(512, 2) void fused_kernel(Params) {
  __shared__ __attribute__((aligned(16))) char smem_raw[L_END];
  LAS char* sm = (LAS char*)smem_raw;
  u64x8 pa_; u64x4 pb_;
  asm volatile("s_load_dwordx16 %0, %2, 0x0\n\ts_load_dwordx8 %1, %2, 0x40" : "=&s"(pa_), "=&s"(pb_) : "s"(__builtin_amdgcn_kernarg_segment_ptr()));
  {
    unsigned long long pc;
    asm volatile("s_getpc_b64 %0" : "=s"(pc));
    if (__builtin_amdgcn_readfirstlane(threadIdx.x) < 128) touch(mkrs((const void*)(pc & ~127ull), CODE_LEN), (threadIdx.x * 4 + ((blockIdx.x >> 3) & 3)) * 128, L_B2 + 1536);
  }
  const unsigned tid = threadIdx.x, lane = tid & 63;
  const unsigned w = __builtin_amdgcn_readfirstlane(tid >> 6);
  const unsigned tg = w & 3, fg = w >> 2;
  const unsigned b = blockIdx.x, xcd = b & 7, bi = b >> 3;
  const unsigned r = (xcd & 1) * 8 + (bi & 7), c = (xcd >> 1) * 4 + (bi >> 3);
  const unsigned l31 = lane & 31, h = lane >> 5, blk = (lane >> 4) & 1, q = (lane & 15) >> 2, p = lane & 3;
  const bool kvwg = (c == 3) || (c == 7);
  const unsigned swz4 = ((lane & 7) ^ (((lane >> 4) & 1) << 2)) << 4;
  unsigned vx0 = (16 * w + (lane >> 3)) * 1152 + (((lane & 7) ^ (lane >> 4)) << 4);
  unsigned vx1 = (16 * w + 8 + (lane >> 3)) * 1152 + (((lane & 7) ^ (4 + (lane >> 4))) << 4);
  unsigned vw1 = (8 * w + (lane >> 3)) * 9216 + swz4;
  unsigned vw2 = (8 * w + (lane >> 3)) * 1152 + swz4;
  unsigned w1col = (288 * c + (c & 1) * 32) * 2, wqcol = (72 * c) & ~15u, xoff = r * 147456;
  const bool isb2 = (w == 1) || (w == 2);
  unsigned vb = lane * 16 + (isb2 ? (w - 1) * 1024 : c * 576), lb = isb2 ? L_B2 + (w - 1) * 1024 : L_B1, vg = (tid >> 1) * 4, vk = tid * 36;
  if (HOIST) { TIE(vx0); TIE(vx1); TIE(vw1); TIE(vw2); TIE(vb); TIE(vg); TIE(vk); asm volatile("" : "+s"(w1col), "+s"(wqcol), "+s"(xoff), "+s"(lb)); }
  asm volatile("s_waitcnt lgkmcnt(0)" : "+s"(pa_), "+s"(pb_));
  const Params P = {(const void*)pa_[0], (const void*)pa_[1], (const void*)pa_[2], (const void*)pa_[3], (const void*)pa_[4], (const void*)pa_[5],
                    (const void*)pa_[6], (const void*)pa_[7], (const void*)pb_[0], (const void*)pb_[1], (void*)pb_[2], (void*)pb_[3]};

  const u32x4 rs_ws = mkrs(P.ws, WS_END);
  const u32x4 rs_x = mkrs((const char*)P.x + xoff, 147456);
  const u32x4 rs_w1 = mkrs(P.w1, 576 * 9216);
  const u32x4 rs_wq = mkrs(P.wq, 576 * 1152);

  unsigned epoch, gw0, gw1 = 0, kc0 = 0, kc1 = 0;
  u32x4 ka0, kb0, ka1, kb1;
  auto issue_p0 = [&](unsigned G, unsigned half) {
    const unsigned slot = (G % 3) * 24576;
    if (half == 0) {
      glds(rs_x, vx0, G * 128, slot + 8192 + w * 2048);
      glds(rs_x, vx1, G * 128, slot + 8192 + w * 2048 + 1024);
    } else {
      glds(rs_w1, vw1, w1col + G * (64 * 9216), slot + w * 1024);
      glds_wq(rs_wq, vw2, wqcol + G * (64 * 1152), L_H + G * 8192 + w * 1024);
    }
  };
  auto issue_slab = [&](unsigned j, unsigned u, unsigned m) {
    if (ABL & 1) return;
    glds(rs_w1, vw1, w1col + 128 * j + (u * 192 + m * 64) * 9216, u * 24576 + m * 8192 + w * 1024);
  };
  if (FIRST0) { issue_p0(0, 0); issue_p0(0, 1); }
  aload1_sc1(epoch, rs_ws, 0u * lane, WS_EPOCH);
  {
    const u32x4 rs_gw = mkrs(P.gw, 1152);
    const u32x4 rs_kv = mkrs(c == 7 ? P.wv : P.wk, 576 * 36);
    aload1(gw0, rs_gw, vg, 0);
    if (w == 0) aload1(gw1, rs_gw, vg, 1024);
    if (kvwg) {
      aload4(ka0, rs_kv, vk, 0); aload4(kb0, rs_kv, vk, 16); aload1(kc0, rs_kv, vk, 32);
      if (w == 0) { aload4(ka1, rs_kv, vk, 512 * 36); aload4(kb1, rs_kv, vk, 512 * 36 + 16); aload1(kc1, rs_kv, vk, 512 * 36 + 32); }
    }
    const u32x4 rs_b = mkrs(isb2 ? P.b2 : P.b1, isb2 ? 1152 : 9216);
    glds(rs_b, vb, 0, lb);
  }
  if (!FIRST0) { issue_p0(0, 0); issue_p0(0, 1); }
  issue_p0(1, 0); issue_p0(1, 1); issue_p0(2, 0); issue_p0(2, 1);
  wait_vm<FIRST0 ? 9 : 13>();
  TIE(epoch); TIE(gw0); TIE(gw1); TIE(kc0); TIE(kc1); TIE(ka0); TIE(kb0); TIE(ka1); TIE(kb1);
  const unsigned tag = __builtin_amdgcn_readfirstlane(epoch) + 1;
  {
    const unsigned sx = ((tid >> 1) & 1) << 6;
    const unsigned ra = L_H + tid * 128;
    const unsigned sh = (tid & 1) * 16;
    u32x2 o0 = {kc0, 0x3f80u | ((gw0 >> sh) << 16)};
    lds_st<u32x2>(sm, ra + (112 ^ sx), o0);
    if (kvwg) { lds_st<u32x4>(sm, ra + (80 ^ sx), ka0); lds_st<u32x4>(sm, ra + (96 ^ sx), kb0); }
    if (w == 0) {
      u32x2 o1 = {kc1, 0x3f80u | ((gw1 >> sh) << 16)};
      lds_st<u32x2>(sm, ra + 65536 + (112 ^ sx), o1);
      if (kvwg) { lds_st<u32x4>(sm, ra + 65536 + (80 ^ sx), ka1); lds_st<u32x4>(sm, ra + 65536 + (96 ^ sx), kb1); }
    }
  }

  const unsigned rot = tg + 2 * fg;
  unsigned xa[4], wa[4], qa[4];
  {
    const unsigned p8 = 4 * blk + p;
    const unsigned bo = p8 < 5 ? ((72 * c) & 15) + 32 * fg + 8 * p8 : (fg == 0 ? 40 + 8 * p8 : (p8 == 5 ? 104 : 112));
    const unsigned sq = (q >> 1) << 2;
#pragma unroll
    for (int e = 0; e < 4; ++e) {
      const unsigned sg = (e + rot) & 3;
      xa[e] = 8192 + (32 * tg + l31) * 128 + (((2 * sg + h) ^ ((l31 >> 1) & 7)) << 4);
      wa[e] = (8 * h + q) * 128 + (((4 * fg + 2 * blk + (p >> 1)) ^ sq) << 4) + 8 * (p & 1) + sg * 2048;
      qa[e] = L_H + (8 * h + q) * 128 + (((bo >> 4) ^ sq) << 4) + (bo & 8) + sg * 2048;
    }
  }
  bf16x8 xf[36], wf[3], qf[3];
  f32x16 acc = zero16(), accq = zero16();
  f32x4 csa = {0.f, 0.f, 0.f, 0.f};
  f32x4 ssa = {0.f, 0.f, 0.f, 0.f}, ssb = ssa;
  const unsigned selu = ((lane & 15) == blk) ? 0x3f803f80u : 0u;
  const bf16x8 sel = asfrag(u32x4{selu, selu, selu, selu});
  const unsigned csw = L_CS + (fg * 4 + tg) * 128 + lane * 4;
  const unsigned csr = L_CS + fg * 512 + l31 * 4;
  float cs0, cs1, cs2, cs3;
  auto p0_read = [&]<int S>() {
    constexpr unsigned G = S >> 2, slot = (G % 3) * 24576;
    xf[S] = ldfrag(sm, xa[S & 3] + slot);
    wf[S % 3] = trfrag(sm, wa[S & 3] + slot, wa[S & 3] + slot + 512);
    if constexpr (G < 8) qf[S % 3] = trfrag(sm, qa[S & 3] + G * 8192, qa[S & 3] + G * 8192 + 512);
    else { const unsigned a = add64k(qa[S & 3]); qf[S % 3] = trfrag(sm, a, a + 512); }
  };
  auto slab_read = [&]<int S>() {
    constexpr unsigned o = (S / 12) * 24576 + ((S % 12) >> 2) * 8192;
    if constexpr (o < 65536) wf[S % 3] = trfrag(sm, wa[S & 3] + o, wa[S & 3] + o + 512);
    else { const unsigned a = add64k(wa[S & 3]); wf[S % 3] = trfrag(sm, a, a + 512); }
  };
  auto cs_write = [&]() { if (lane < 16) { lds_st<float>(sm, csw, csa[0]); lds_st<float>(sm, csw + 64, csa[1]); } };
  auto cs_read = [&]() { cs0 = lds_ld<float>(sm, csr); cs1 = lds_ld<float>(sm, csr + 128); cs2 = lds_ld<float>(sm, csr + 256); cs3 = lds_ld<float>(sm, csr + 384); };

  if (PRIO & 2) if (fg) __builtin_amdgcn_s_setprio(1);
  sync_vm<8>();
  STAMP(1);
  p0_read.template operator()<0>(); p0_read.template operator()<1>();
  Unroll<0, 36>::run([&]<int S>() {
    constexpr int G = (S >> 2) + 1;
    SB();
    if constexpr ((S & 3) == 2) {
      if constexpr (G == 9) cs_write();
      sync_vm<G <= 7 ? 4 : 3>();
      if constexpr (G == 9) cs_read();
    }
    if constexpr (S + 2 < 36) p0_read.template operator()<S + 2>(); else slab_read.template operator()<S + 2 - 36>();
    SB();
    acc = mfma32(wf[S % 3], xf[S], acc);
    SB();
    if constexpr ((S & 3) >= 2) {
      if constexpr (G + 2 <= 8) issue_p0(G + 2, S & 1);
      else { issue_slab(1, G + 2 - 9, S & 1); if constexpr (S & 1) issue_slab(1, G + 2 - 9, 2); }
    }
    SB();
    accq = mfma32(qf[S % 3], xf[S], accq);
    if constexpr ((S & 3) == 0) csa = mfma16(sel, wf[S % 3], csa);
    if constexpr ((S & 3) < 2) {
      const u32x4 u = __builtin_bit_cast(u32x4, xf[S]);
      const s16x4 lo = __builtin_bit_cast(s16x4, u32x2{u[0], u[1]}), hi = __builtin_bit_cast(s16x4, u32x2{u[2], u[3]});
      ssa = __builtin_amdgcn_mfma_f32_4x4x4bf16_1k(lo, lo, ssa, 0, 0, 0);
      ssb = __builtin_amdgcn_mfma_f32_4x4x4bf16_1k(hi, hi, ssb, 0, 0, 0);
    }
  });
  SB();
  STAMP(2);
  const unsigned tok = 32 * tg + l31;
  {
    const float qs = 0.34004497f;
    const unsigned qb = L_QR + fg * 5120 + tok * 40 + 8 * h;
    lds_st<u32x2>(sm, qb, u32x2{cvtpk(accq[0] * qs, accq[1] * qs), cvtpk(accq[2] * qs, accq[3] * qs)});
    lds_st<u32x2>(sm, qb + 16, u32x2{cvtpk(accq[4] * qs, accq[5] * qs), cvtpk(accq[6] * qs, accq[7] * qs)});
    ssa += ssb;
    const float ss = sum32((p & 2) ? ((p & 1) ? ssa[3] : ssa[2]) : ((p & 1) ? ssa[1] : ssa[0]));
    if (h == 0) {
      lds_st<u32x2>(sm, qb + 32, u32x2{cvtpk(accq[8] * qs, accq[9] * qs), cvtpk(accq[10] * qs, accq[11] * qs)});
      lds_st<float>(sm, L_SSQ + fg * 512 + tok * 4, ss);
      if (fg == 1) { lds_st<float>(sm, L_SUM + tok * 4, accq[14]); lds_st<float>(sm, L_GL + tok * 4, accq[15]); }
    }
    if (kvwg) {
      const unsigned kb = L_KVST + tok * 40;
      if (h == 1) lds_st<u32x2>(sm, kb + fg * 24, u32x2{cvtpk(accq[8], accq[9]), cvtpk(accq[10], accq[11])});
      if (fg == 0 || h == 0) lds_st<u32x2>(sm, kb + (fg ? 32 : 8 + 8 * h), u32x2{cvtpk(accq[12], accq[13]), cvtpk(accq[14], accq[15])});
    }
  }
  sync_lds();
  float rho, sig, gate;
  bf16x8 bext;
  Gelu gl;
  {
    const float s2 = lds_ld<float>(sm, L_SSQ + tok * 4) + lds_ld<float>(sm, L_SSQ + 512 + tok * 4);
    const float mu = lds_ld<float>(sm, L_SUM + tok * 4) * (1.f / 576.f);
    const float lg = lds_ld<float>(sm, L_GL + tok * 4);
    const float var = fmaxf(s2 * (1.f / 576.f) - mu * mu, 0.f);
    sig = __builtin_amdgcn_sqrtf(var + 1e-5f);
    rho = __builtin_amdgcn_rsqf(var + 1e-5f);
    gate = __builtin_amdgcn_rcpf(1.f + __builtin_amdgcn_exp2f(-1.44269504f * lg));
    const float mh = rbf(-mu), ml = -mu - mh, sh = rbf(sig), sm1 = rbf(sig - sh), sl = sig - sh - sm1;
    u32x4 be = {cvtpk(mh, ml), cvtpk(mh, sh), cvtpk(sm1, sl), 0u};
    if (h) be = u32x4{0u, 0u, 0u, 0u};
    bext = asfrag(be);
    gl.k1 = -2.302208198f * rho; gl.k2 = -0.102943240f * rho * rho * rho;
    gl.k3 = fminf(sig * __builtin_amdgcn_rcpf(1.f - gate), 1e30f);
  }
  if (kvwg && fg == 0) {
    const unsigned T = 4 * r + tg;
    if (c == 3) {
      const unsigned kb = L_KVST + tok * 40;
      const u32x2 a0 = lds_ld<u32x2>(sm, kb + 16 * h), a1 = lds_ld<u32x2>(sm, kb + 16 * h + 8);
      const unsigned e2 = lds_ld<unsigned>(sm, kb + 32);
      float n2 = dot2(a1[1], dot2(a1[0], dot2(a0[1], dot2(a0[0], dot2(h ? 0u : e2, 0.f)))));
      PAD(n2, 2);
      unsigned nm = __float_as_uint(sum32(n2));
      nm = umax(nm, (unsigned)__builtin_amdgcn_update_dpp(0, nm, 0x128, 0xf, 0xf, false));
      nm = umax(nm, (unsigned)__builtin_amdgcn_update_dpp(0, nm, 0x124, 0xf, 0xf, false));
      nm = umax(nm, (unsigned)__builtin_amdgcn_update_dpp(0, nm, 0x122, 0xf, 0xf, false));
      nm = umax(nm, (unsigned)__builtin_amdgcn_update_dpp(0, nm, 0x121, 0xf, 0xf, false));
      nm = umax(nm, (unsigned)__builtin_amdgcn_readlane(nm, 16));
      store4_sc1(u32x4{a0[0], a0[1], a1[0], a1[1]}, rs_ws, lane * 16, WS_KV + T * 3072);
      if (h == 0) store1_sc1(e2, rs_ws, lane * 4, WS_KV + T * 3072 + 1024 + 384);
      if (lane == 0) store1_sc1(nm, rs_ws, lane, WS_KNORM + T * 4);
    } else {
      const unsigned fill = l31 == 18 ? 0x3f803f80u : 0u, va = L_KVST + (32 * tg + 8 * h + q) * 40 + 32 * blk + 8 * p;
#pragma unroll
      for (int u = 0; u < 2; ++u) {
        u32x4 v = __builtin_bit_cast(u32x4, trfrag(sm, va + u * 640, va + u * 640 + 160));
#pragma unroll
        for (int e = 0; e < 4; ++e) v[e] = l31 < 18 ? v[e] : fill;
        if (u == 1 || lane < 24 || lane >= 32) store4_sc1(v, rs_ws, lane * 16, WS_KV + T * 3072 + 1024 + u * 1024);
      }
    }
  }
  {
    const unsigned ob = WS_O + (4 * r + tg) * 36864;
    store4_sc1(u32x4{~0u, ~0u, ~0u, ~0u}, rs_ws, lane * 16, ob + (2 * c + fg) * 1024);
    store2_sc1(u32x2{~0u, ~0u}, rs_ws, l31 * 8, ob + 32768 + c * 256);
  }
  STAMP(3);

  const unsigned hbase = L_H + tg * 18432 + lane * 16;
  const unsigned b1a = L_B1 + l31 * 2;
  const unsigned vhs = (16 * w + (lane >> 2)) * 9216 + (lane & 3) * 16;
  const unsigned hcol = (288 * c + ((c & 1) ? 0 : 256)) * 2;
  const unsigned hsb = (8 * h + q) * 64 + 32 * blk + 8 * p;
  const u32x4 rs_w2 = mkrs((const char*)P.w2 + c * (288 * 1152), 288 * 1152);
  auto issue_w2 = [&](unsigned j, unsigned slot) {
    if (ABL & 2) return;
#pragma unroll
    for (unsigned m = 0; m < 4; ++m) glds(rs_w2, vw2, 128 * j + m * (64 * 1152), slot + m * 8192 + w * 1024);
    glds_half(rs_w2, vw2, 128 * j + (256 - 4 * w) * 1152, slot + 32768 + w * 512);
  };
  auto issue_w2p = [&](unsigned j, unsigned slot, unsigned m) {
    if (ABL & 2) return;
    if (m < 4) glds(rs_w2, vw2, 128 * j + m * (64 * 1152), slot + m * 8192 + w * 1024);
    else glds_half(rs_w2, vw2, 128 * j + (256 - 4 * w) * 1152, slot + 32768 + w * 512);
  };
  const unsigned w2i = w * 9 + (lane < 9 ? lane : 8);
  const unsigned w2l = ((bi & 7) * 36 + (w2i >> 1)) * 1152 + (w2i & 1) * 128;
  f32x16 ac1[2];
  ac1[0] = acc;
  const unsigned ps0 = (c & 1) + fg;
  const unsigned hw0 = hbase + ps0 * 2048, b1w = b1a + ps0 * 64;
  unsigned ha[4];
#pragma unroll
  for (int e = 0; e < 4; ++e) ha[e] = hsb + ((e + rot) & 3) * 1024;
  bf16x8 aext = make_aext((cs0 + cs1) + (cs2 + cs3), lds_ld<unsigned short>(sm, b1w));
  unsigned b1n;
  Unroll<1, 4>::run([&]<int J>() {
    f32x16& pa = ac1[(J - 1) & 1];
    f32x16& ac = ac1[J & 1];
    Unroll<0, 36>::run([&]<int S>() {
      SB();
      if constexpr (S == 0) pa = mfma32(aext, bext, pa);
      if constexpr (S % 12 == 10) {
        if constexpr (S == 34) cs_write();
        if (!(ABL & 512) || J == 3) sync_vm<(S == 34 && J == 3) ? 2 : (S == 10 ? (J == 1 ? 5 : 3) : 4)>();
        if constexpr (S == 34) { cs_read(); b1n = lds_ld<unsigned short>(sm, b1w + J * 128); }
      }
      if constexpr (S + 2 < 36) slab_read.template operator()<S + 2>();
      else if constexpr (J < 3) slab_read.template operator()<S + 2 - 36>();
      else wf[S - 34] = trfrag(sm, ha[S - 34], ha[S - 34] + 256);
      SB();
      if constexpr (S == 0) ac = mfma32(wf[0], xf[0], zero16()); else ac = mfma32(wf[S % 3], xf[S], ac);
      SB();
      constexpr int SP = (S + 2) % 12, SG = (S + 2) / 12 - 1;
      if constexpr (SG >= 0 && SP <= 2) {
        if constexpr (J < 3) { issue_slab(J + 1, SG, SP); if constexpr (SG == 2 && SP == 1) issue_slab(J + 1, 2, 2); }
        else if constexpr (SG == 0) glds(rs_w1, vhs, hcol + SP * (128 * 9216), (w + 8 * SP) * 1024);
        else if constexpr (SG == 1 && SP == 0) glds(rs_w1, vhs, hcol + 384 * 9216, (24 + w) * 1024);
        else if constexpr (SG == 1 && SP == 1) glds(rs_w1, vhs, hcol + (512 - 16 * (w & 4)) * 9216, (32 + (w & 3)) * 1024);
        else if constexpr (SG == 2 && SP == 0) { issue_w2p(0, 36864, 0); issue_w2p(0, 36864, 1); }
        else if constexpr (SG == 2 && SP == 1) { issue_w2p(0, 36864, 2); issue_w2p(0, 36864, 3); issue_w2p(0, 36864, 4); }
      }
      if constexpr ((S == 13 || S == 25) && 2 * J + (S == 25) < 7) {
        constexpr unsigned j0 = 4 * (J - 1) + (S == 25 ? 2 : 0);
        touch(rs_w2, w2l + (j0 < 8 ? j0 * 128 : 1024 - (w2i & 1) * 128), L_B2 + 1536);
      }
      if constexpr (S == 25 && J == 1) if (kvwg && w == 0) set_flag(rs_ws, WS_KVFLAG + (r * 2 + (c >> 2)) * 4, tag);
      SB();
      if constexpr (!(ABL & 1024)) gelu_step<S>(gl, pa, sm, hw0 + (J - 1) * 4096); else if constexpr (S == 35) TIE(pa);
      if (PRIO & 4) SB();
      if constexpr (S == 0) csa = mfma16(sel, wf[0], f32x4{0.f, 0.f, 0.f, 0.f});
      else if constexpr ((S & 3) == 0 && !(ABL & 2048)) csa = mfma16(sel, wf[S % 3], csa);
      if constexpr (S == 35) aext = make_aext((cs0 + cs1) + (cs2 + cs3), b1n);
    });
  });
  SB();
  STAMP(4);
  ac1[1] = mfma32(aext, bext, ac1[1]);
  constexpr int NT = (WOT & 1) + (WOT >> 1 & 1) + (WOT >> 2 & 1);
  {
    const unsigned ti = bi < 24 ? bi : bi - 24, T2 = xcd * 4 + ti / 6;
    if (WOT & 1) touch(mkrs(P.wo, 576 * 1152), ((bi * 8 + w) * 21 + (lane < 21 ? lane : 20)) * 128, L_B2 + 1536);
    if (WOT & 2) touch(mkrs(P.out, 2048 * 1152), T2 * (64 * 1152) + (lane & 1) * (63 * 1152), L_B2 + 1536);
    if (WOT & 4) touch(rs_ws, WS_PART + ((lane & 2) ? (2 * T2 + (lane & 1)) : (4 * r + 3 * (lane & 1))) * (18 * 32768) + (lane & 1) * (17 * 32768), L_B2 + 1536);
  }
  if (fg == 0) {
    Unroll<0, 36>::run([&]<int S>() {
      SB();
      if constexpr (S == 22) sync_vm<5 + NT>();
      if constexpr (S == 34) { cs_write(); sync_lds(); cs_read(); b1n = lds_ld<unsigned short>(sm, b1a + ((c & 1) ? 0 : 512)); }
      if constexpr (S + 2 < 36) wf[(S + 2) % 3] = trfrag(sm, ha[(S + 2) & 3] + ((S + 2) >> 2) * 4096, ha[(S + 2) & 3] + ((S + 2) >> 2) * 4096 + 256);
      SB();
      if constexpr (S == 0) ac1[0] = mfma32(wf[0], xf[0], zero16()); else ac1[0] = mfma32(wf[S % 3], xf[S], ac1[0]);
      if constexpr (S == 0) csa = mfma16(sel, wf[0], f32x4{0.f, 0.f, 0.f, 0.f});
      else if constexpr ((S & 3) == 0) csa = mfma16(sel, wf[S % 3], csa);
      gelu_step<S>(gl, ac1[1], sm, hw0 + 3 * 4096);
    });
    SB();
    ac1[0] = mfma32(make_aext((cs0 + cs1) + (cs2 + cs3), b1n), bext, ac1[0]);
  } else {
    Unroll<0, 36>::run([&]<int S>() { gelu_step<S>(gl, ac1[1], sm, hw0 + 3 * 4096); });
    sync_vm<5 + NT>();
    sync_lds();
  }
  if (HFP) sync_vm<NT>(); else sync_lds();
  issue_w2(1, 0);
  STAMP(5);

  {
  unsigned tid = threadIdx.x;
  TIE(tid);
  const unsigned lane = tid & 63, l31 = lane & 31, h = lane >> 5, blk = (lane >> 4) & 1, q = (lane & 15) >> 2, p = lane & 3;
  const unsigned tok = 32 * tg + l31;
  bf16x8 hf[18];
  const unsigned late = (c & 1) ? 0 : 16, first = (c & 1) ? 2 : 0;
  const unsigned hlate = (HFP ? (tg < 2 ? L_SSQ + tg * 2048 : L_H + tg * 18432 + late * 1024) : L_RING + tg * 2048) + lane * 16;
  const unsigned hfa = hbase + first * 1024;
  if (!HFP) {
#pragma unroll
    for (int u = 0; u < 16; ++u) hf[u] = ldfrag(sm, hfa + u * 1024);
    sync_vm<5>();
    issue_w2(2, 73728);
  }
  const unsigned w2a = (4 * h + q) * 128 + (((4 * fg + 2 * blk + (p >> 1)) ^ ((q >> 1) << 2)) << 4) + 8 * (p & 1);
  const unsigned pso = WS_PART + (4 * r + tg) * (18 * 32768) + fg * 32768 + c * 2048;
  unsigned kvf, knorm;
  const unsigned vkv0 = w * 1024 + lane * 16 + (w >= 6 ? 32 * 3072 - 6144 : 0);
  const unsigned vkv1 = 8192 + w * 512 + lane * 16 + 32 * 3072 - 6144;
  auto issue_kv = [&](unsigned st, unsigned slot) {
    glds_sc1(rs_ws, vkv0, WS_KV + st * 6144, L_RING + slot + w * 1024);
    glds_half_sc1(rs_ws, vkv1, WS_KV + st * 6144, L_RING + slot + 8192 + w * 512);
  };
  const unsigned ti = bi < 24 ? bi : bi - 24;
  const unsigned T2 = xcd * 4 + ti / 6, nb = ti % 6, tg2 = w & 1, kq = w >> 1;
  const u32x4 rs_wo = mkrs(P.wo, 576 * 1152);
  const unsigned plo = WS_PART + ((2 * T2 + tg2) * 18 + 3 * nb) * 32768 + kq * 8192;
  bf16x8 qA[2], qB[2], idA[2];
  f32x16 O0, O1, pm[3], Sa, Sb;
  u32x4 qv[2], pf[2][2], kB0 = {0u, 0x3f803f80u, 0x3f80u, 0u}, kB1 = kB0;
  bf16x8 kA0, kA1, v00, v01, v10, v11;
  unsigned e2[2], pflg, pfo, wov[3], kva, kba, idv, idi;
  u32x4 idu[2];
  float qn2[2], M[2];
  auto issue_wo = [&]<int S>() {
    if (ABL & 16) return;
    const unsigned back = (S == 13 && w >= 4) ? 1 : 0;
    glds(rs_wo, wov[S % 3], nb * 192 + (S / 3 - back) * (128 * 1152), (S * 8 - back * 24 + w) * 1024);
  };
  auto att_setup = [&]<int J, int U>() {
    if constexpr (J == 2 && U == 12) {
#pragma unroll
      for (int hd = 0; hd < 2; ++hd) {
        const unsigned qb = L_QR + hd * 5120 + tok * 40 + 4 * hd;
#pragma unroll
        for (int i = 0; i < 4; ++i) qv[hd][i] = lds_ld<unsigned>(sm, qb + 16 * h + 4 * i);
        e2[hd] = lds_ld<unsigned>(sm, qb + 32);
      }
    }
    if constexpr (J == 2 && (U == 14 || U == 15)) {
      constexpr int hd = U - 14;
      qA[hd] = asfrag(qv[hd]);
      if (h) e2[hd] = 0;
      float n2 = dot2(e2[hd], 0.f);
#pragma unroll
      for (int i = 0; i < 4; ++i) n2 = dot2(qv[hd][i], n2);
      PAD(n2, 2);
      qn2[hd] = sum32(n2);
      TIE(qn2[hd]);
    }
    if constexpr (J == 3 && U == 12) {
      idv = ((l31 >> 2) & 1) == h ? 0x3f80u << (16 * (l31 & 1)) : 0u;
      idi = (l31 >> 3) * 2 + ((l31 >> 1) & 1);
      TIE(idv); TIE(idi);
    }
    if constexpr (J == 3 && U >= 13 && U <= 16) {
      constexpr int k = 2 * (U - 13);
      idu[k >> 2][k & 3] = idi == k ? idv : 0u; idu[k >> 2][(k & 3) + 1] = idi == k + 1 ? idv : 0u;
      if constexpr (k & 2) { TIE(idu[k >> 2]); idA[k >> 2] = asfrag(idu[k >> 2]); }
    }
    if constexpr (J == 4 && U >= 13 && U <= 15) {
      constexpr int i = U - 13;
      const unsigned idx = (i * 8 + w) * 64 + lane, row = idx / 12;
      wov[i] = row * 1152 + (idx - row * 12) * 16;
      TIE(wov[i]);
    }
    if constexpr (J == 4 && U == 16) {
      pfo = WS_PFLAG + ((T2 >> 1) * 32 + 4 * kq + (lane & 3)) * 4;
      kva = L_RING + fg * 6144 + lane * 16; kba = L_RING + fg * 6144 + 1408 + l31 * 4;
      TIE(pfo); TIE(kva); TIE(kba);
    }
    if constexpr (J == 8 && U == 0) {
      wait_vm<6>();
      TIE(knorm);
      knorm = umax(knorm, (unsigned)__builtin_amdgcn_update_dpp(0, knorm, 0x128, 0xf, 0xf, false));
      knorm = umax(knorm, (unsigned)__builtin_amdgcn_update_dpp(0, knorm, 0x124, 0xf, 0xf, false));
      knorm = umax(knorm, (unsigned)__builtin_amdgcn_update_dpp(0, knorm, 0x122, 0xf, 0xf, false));
      knorm = umax(knorm, (unsigned)__builtin_amdgcn_update_dpp(0, knorm, 0x121, 0xf, 0xf, false));
      TIE(knorm);
    }
    if constexpr (J == 8 && U == 1) {
      const float kmax = __uint_as_float(umax(umax((unsigned)__builtin_amdgcn_readlane(knorm, 0), (unsigned)__builtin_amdgcn_readlane(knorm, 16)),
                                             umax((unsigned)__builtin_amdgcn_readlane(knorm, 32), (unsigned)__builtin_amdgcn_readlane(knorm, 48))));
      M[0] = __builtin_amdgcn_sqrtf(qn2[0] * kmax) * 1.01f;
      M[1] = __builtin_amdgcn_sqrtf(qn2[1] * kmax) * 1.01f;
      if (__builtin_expect(!__all(M[0] <= 50.f && M[1] <= 50.f), 0)) {
        const bf16x8 z0 = asfrag(u32x4{e2[0], 0u, 0u, 0u}), z1 = asfrag(u32x4{e2[1], 0u, 0u, 0u});
        float m0 = -3.0e38f, m1 = -3.0e38f;
#pragma unroll 1
        for (unsigned T = 0; T < 64; ++T) {
          u32x4 kau; unsigned kbu;
          aload4_sc1(kau, rs_ws, lane * 16, WS_KV + T * 3072);
          aload1_sc1(kbu, rs_ws, l31 * 4, WS_KV + T * 3072 + 1408);
          wait_vm<0>();
          TIE(kau); TIE(kbu);
          const bf16x8 ka = asfrag(kau), kb = asfrag(u32x4{kbu, 0u, 0u, 0u});
          const f32x16 s0 = mfma32(kb, z0, mfma32(ka, qA[0], zero16())), s1 = mfma32(kb, z1, mfma32(ka, qA[1], zero16()));
#pragma unroll
          for (int i = 0; i < 16; ++i) { m0 = fmaxf(m0, s0[i]); m1 = fmaxf(m1, s1[i]); }
        }
        M[0] = fmaxf(m0, swap32(m0)); M[1] = fmaxf(m1, swap32(m1));
      }
      TIE(M[0]); TIE(M[1]);
    }
    if constexpr (J == 8 && (U == 2 || U == 3)) {
      constexpr int hd = U - 2;
      const float m = h ? 0.f : -M[hd], a = rbf(m), bq = rbf(m - a), cq = m - a - bq;
      u32x4 v = {e2[hd], cvtpk(a, bq), cvtpk(cq, 0.f), 0u};
      TIE(v);
      qB[hd] = asfrag(v);
    }
    if constexpr (J == 7 && U >= 12 && U <= 15) { for (int i = 0; i < 4; ++i) O0[4 * (U - 12) + i] = 0.f; if constexpr (U == 15) TIE(O0); }
    if constexpr (J == 8 && U >= 12 && U <= 15) { for (int i = 0; i < 4; ++i) O1[4 * (U - 12) + i] = 0.f; if constexpr (U == 15) TIE(O1); }
    if constexpr (J == 8 && U == 16) { issue_wo.template operator()<0>(); issue_kv(2, 24576); }
    if constexpr (J == 8 && U == 17) Sa = mfma32(asfrag(kB0), qB[0], mfma32(kA0, qA[0], zero16()));
  };
  unsigned pk0, pk1, pk2;
  auto part_epi = [&]<int E>(const f32x16& pa, unsigned jprev) {
    const unsigned pk = cvtpk(pa[2 * E], pa[2 * E + 1]);
    if constexpr ((E & 3) == 0) pk0 = pk;
    if constexpr ((E & 3) == 1) pk1 = pk;
    if constexpr ((E & 3) == 2) pk2 = pk;
    if constexpr ((E & 3) == 3 && (ABL & 4)) { unsigned t = pk; TIE(pk0); TIE(pk1); TIE(pk2); TIE(t); }
    if constexpr ((E & 3) == 3 && !(ABL & 4)) store4_sc1(u32x4{pk0, pk1, pk2, pk}, rs_ws, lane * 16, pso + jprev * 65536 + (E >> 2) * 1024);
  };
  {
    const unsigned w2s[3] = {w2a + first * 2048 + 36864, w2a + first * 2048, w2a + first * 2048 + 73728};
    const unsigned w2t[3] = {w2a + late * 2048 + 36864, w2a + late * 2048, w2a + late * 2048 + 73728};
    f32x16 ac2[2];
    constexpr int PD = G2PD, NW = PD + 1;
    bf16x8 wg[NW];
#pragma unroll
    for (int i = 0; i < PD; ++i) { if (HFP) hf[i] = ldfrag(sm, hfa + i * 1024); wg[i] = trfrag(sm, w2s[0] + i * 2048, w2s[0] + i * 2048 + 1024); }
#pragma unroll
    for (int i = PD; i < HFP; ++i) hf[i] = ldfrag(sm, hfa + i * 1024);
    auto slab = [&]<int J, bool LATE>() {
      STAMP2(16 + J);
      constexpr unsigned slot = J % 3 == 0 ? 36864 : (J % 3 == 1 ? 0 : 73728);
      constexpr unsigned pslot2 = (J + 2) % 3 == 0 ? 36864 : ((J + 2) % 3 == 1 ? 0 : 73728);
      Unroll<0, 18>::run([&]<int U>() {
        SB();
        if constexpr (U == 18 - PD && J < 8) sync_vm<J == 0 ? 5 : (J == 1 ? 7 : (J == 5 ? 10 : 9))>();
        if constexpr (U == 16 && J == 8) {
          sync_vm<6>();
          kA0 = ldfrag(sm, kva); kB0[0] = lds_ld<unsigned>(sm, kba); v00 = ldfrag(sm, kva + 1024); v01 = ldfrag(sm, kva + 2048);
        }
        if constexpr (U == 13 && J == 0) { sync_lds(); hf[16] = ldfrag(sm, hlate); hf[17] = ldfrag(sm, hlate + 1024); }
        constexpr int V = U + PD, VI = (18 * J + V) % NW, UI = (18 * J + U) % NW;
        if constexpr (V < 16) wg[VI] = trfrag(sm, w2s[J % 3] + V * 2048, w2s[J % 3] + V * 2048 + 1024);
        else if constexpr (V < 18) wg[VI] = trfrag(sm, w2t[J % 3] + (V - 16) * 2048, w2t[J % 3] + (V - 16) * 2048 + 1024);
        else if constexpr (J < 8) wg[VI] = trfrag(sm, w2s[(J + 1) % 3] + (V - 18) * 2048, w2s[(J + 1) % 3] + (V - 18) * 2048 + 1024);
        if constexpr (HFP && J == 0 && U + HFP < 16) hf[U + HFP] = ldfrag(sm, hfa + (U + HFP) * 1024);
        SB();
        if (PRIO & 1) __builtin_amdgcn_s_setprio(1);
        if constexpr (U == 0) ac2[J & 1] = mfma32(wg[UI], hf[0], zero16()); else ac2[J & 1] = mfma32(wg[UI], hf[U], ac2[J & 1]);
        if (PRIO & 1) __builtin_amdgcn_s_setprio(0);
        SB();
        if constexpr (HFP && J == 0 && U >= 13 && U <= 15) { issue_w2p(2, 73728, 2 * (U - 13)); if constexpr (U < 15) issue_w2p(2, 73728, 2 * (U - 13) + 1); }
        if constexpr (U >= 16 && J <= 5) issue_w2p(J + 3, slot, U - 16);
        if constexpr (U <= 2 && J >= 1 && J <= 6) issue_w2p(J + 2, pslot2, U + 2);
        if constexpr (U == 3 && J == 5) aload1_sc1(kvf, rs_ws, l31 * 4, WS_KVFLAG);
        if constexpr (U == 17 && J == 6) {
          TIE(kvf);
          if (__builtin_expect(!__all(kvf == tag), 0)) spin_flags(P.ws, WS_KVFLAG + l31 * 4, true, tag);
          aload1_sc1(knorm, rs_ws, lane * 4, WS_KNORM);
          issue_kv(0, 0); issue_kv(1, 12288);
        }
        if constexpr (U >= 4 && U < 12 && J > 0) part_epi.template operator()<U - 4>(ac2[(J + 1) & 1], J - 1);
        if constexpr (LATE && U < 12) { gelu_step<3 * U>(gl, ac1[0], sm, hlate); gelu_step<3 * U + 1>(gl, ac1[0], sm, hlate); gelu_step<3 * U + 2>(gl, ac1[0], sm, hlate); }
        att_setup.template operator()<J, U>();
        if constexpr (U == 12) {
          if (__builtin_expect(((2 * J + fg) & 15) == c, 0)) {
            const unsigned bv = lds_ld<unsigned short>(sm, L_B2 + (2 * J + fg) * 64 + l31 * 2);
            const float og = h ? 0.f : 1.f - gate, g0 = rbf(og), g1 = rbf(og - g0);
            u32x4 gs = {cvtpk(g0, g1), cvtpk(og - g0 - g1, 0.f), 0u, 0u};
            PAD(gs, 1);
            ac2[J & 1] = mfma32(asfrag(u32x4{bv | bv << 16, bv, 0u, 0u}), asfrag(gs), ac2[J & 1]);
          }
        }
      });
    };
    if (fg == 0) slab.template operator()<0, true>(); else slab.template operator()<0, false>();
    Unroll<1, 9>::run([&]<int J>() { slab.template operator()<J, false>(); });
    SB();
    PAD(ac2[0], 11);
    Unroll<0, 8>::run([&]<int E>() { part_epi.template operator()<E>(ac2[0], 8); });
  }
  STAMP(6);

  auto expo = [&](f32x16& S, int a) {
#pragma unroll
    for (int i = 0; i < 4; ++i) if (!(ABL & 64)) S[a + i] = __builtin_amdgcn_exp2f(S[a + i]);
  };
  auto pv = [&](f32x16& O, const bf16x8& va, const f32x16& S, int u) {
    f32x8 x;
#pragma unroll
    for (int i = 0; i < 8; ++i) x[i] = S[8 * u + i];
    if (!(ABL & 128)) O = __builtin_amdgcn_smfmac_f32_32x32x32_bf16(va, __builtin_bit_cast(bf16x16, x), O, 0xDDDD, 0, 0);
  };
  auto unit = [&](f32x16& C, f32x16& O, const bf16x8& va, const bf16x8& vb, f32x16& N, const bf16x8& ka, const u32x4& kb, int hd, bool qk, auto&& side) {
    expo(C, 0); SB();
    if (qk) N = mfma32(ka, qA[hd], zero16());
    side(0); SB();
    expo(C, 4); SB();
    if (qk) N = mfma32(asfrag(kb), qB[hd], N);
    side(1); SB();
    expo(C, 8); SB();
    pv(O, va, C, 0);
    side(2); SB();
    expo(C, 12); SB();
    pv(O, vb, C, 1);
    side(3); SB();
  };
  auto pred = [&]<int I>() {
    TIE(pf[I & 1][0]); TIE(pf[I & 1][1]);
    if constexpr ((I & 3) == 0) pm[I >> 2] = mfma32(idA[0], asfrag(pf[I & 1][0]), zero16()); else pm[I >> 2] = mfma32(idA[0], asfrag(pf[I & 1][0]), pm[I >> 2]);
    pm[I >> 2] = mfma32(idA[1], asfrag(pf[I & 1][1]), pm[I >> 2]);
  };
  bf16x8 wo[27];
  unsigned wm, wr;
  auto wo_read = [&]<int I>() {
    constexpr int a = I / 9, i = I % 9;
    if constexpr (i < 8) wo[I] = trfrag(sm, wm + i * 3456 + a * 64, wm + i * 3456 + a * 64 + 1536);
    else wo[I] = trfrag(sm, wr + a * 64, wr + a * 64 + 6912);
  };
  auto wo_side = [&]<int ST, int K>(int i) {
    if constexpr (ST == 15) {
      if constexpr (K == 1) if (i == 0) {
        wm = (144 * kq + 4 * h + q) * 192 + 32 * blk + 8 * p;
        wr = (18 * (8 * kq + 4 * h + (q >> 1)) + 16 + (q & 1)) * 192 + 32 * blk + 8 * p;
      }
      Unroll<0, WOPRE>::run([&]<int I>() { if (4 * K + i == 5 + I * 11 / (WOPRE ? WOPRE : 1)) wo_read.template operator()<I>(); });
    }
  };
  STAMP(7);
  Unroll<0, 16>::run([&]<int ST>() {
    STAMP2(ST);
    constexpr int SN = ST + 1;
    constexpr unsigned so = (ST % 3) * 12288, sn = (SN % 3) * 12288;
    SB();
    unit(Sa, O0, v00, v01, Sb, kA0, kB0, 1, true, [&](int i) {
      if (i == 0) { kA1 = ldfrag(sm, kva + so + 3072); kB1[0] = lds_ld<unsigned>(sm, kba + so + 3072); }
      if (i == 1) { v10 = ldfrag(sm, kva + so + 4096); v11 = ldfrag(sm, kva + so + 5120); }
    });
    unit(Sb, O1, v00, v01, Sa, kA1, kB1, 0, true, [&](int i) { wo_side.template operator()<ST, 1>(i); });
    if constexpr (SN < 16 && !(ABL & 32)) sync_vm<SN == 1 ? 9 : (SN == 2 ? 3 : (SN == 3 ? 3 : (SN == 4 ? 3 : (SN <= 14 ? 5 : 2))))>();
    SB();
    unit(Sa, O0, v10, v11, Sb, kA1, kB1, 1, true, [&](int i) {
      wo_side.template operator()<ST, 2>(i);
      if constexpr (SN < 16) {
        if (i == 0) { kA0 = ldfrag(sm, kva + sn); kB0[0] = lds_ld<unsigned>(sm, kba + sn); }
        if (i == 1) { v00 = ldfrag(sm, kva + sn + 1024); v01 = ldfrag(sm, kva + sn + 2048); }
        if (i == 2) {
          if constexpr (SN == 2) if (w == 0) set_flag(rs_ws, WS_PFLAG + (r * 32 + c) * 4, tag);
          if constexpr (SN == 3) aload1_sc1(pflg, rs_ws, pfo, 0);
          if constexpr (SN == 4) {
            TIE(pflg);
            if (!(ABL & 256)) if (__builtin_expect(!__all(pflg == tag), 0)) spin_flags(P.ws, pfo, true, tag);
          }
          if constexpr (SN >= 6) pred.template operator()<SN - 6>();
        }
        if (i == 3) {
          if constexpr (SN >= 4 && !(ABL & 8)) {
            constexpr unsigned I = SN - 4;
            aload4_sc1(pf[I & 1][0], rs_ws, lane * 16, plo + (I >> 2) * 32768 + (I & 3) * 2048);
            aload4_sc1(pf[I & 1][1], rs_ws, lane * 16, plo + (I >> 2) * 32768 + (I & 3) * 2048 + 1024);
          }
        }
      }
    });
    unit(Sb, O1, v10, v11, Sa, kA0, kB0, 0, SN < 16, [&](int i) {
      wo_side.template operator()<ST, 3>(i);
      if constexpr (SN <= 13) {
        if (i == 0) issue_wo.template operator()<SN>();
        if (i == 1) issue_kv(SN + 2, so);
      }
    });
  });
  STAMP(8);
  {
    const unsigned xw = L_RING + 12288 + w * 3072 + lane * 16, xr = L_RING + 12288 + (w ^ 4) * 3072 + lane * 16, xe = L_QR + tg * 1024 + lane * 16;
    const unsigned ob = WS_O + (4 * r + tg) * 36864;
    auto fin = [&]<int FG>(const f32x16& keep, const f32x16& send) {
      lds_st<f32x4>(sm, xw, f32x4{send[0], send[1], send[2], send[3]});
      lds_st<f32x4>(sm, xw + 1024, f32x4{send[4], send[5], send[6], send[7]});
      lds_st<f32x4>(sm, xw + 2048, f32x4{send[8], send[9], send[10], send[11]});
      if constexpr (FG) lds_st<f32x4>(sm, xe, f32x4{keep[8], keep[9], keep[10], keep[11]});
      sync_vm<0>();
      const f32x4 c2 = lds_ld<f32x4>(sm, xr + 2048), c0 = lds_ld<f32x4>(sm, xr), c1 = lds_ld<f32x4>(sm, xr + 1024);
      f32x4 ce;
      if constexpr (!FG) ce = lds_ld<f32x4>(sm, xe);
      const float sc = gate * __builtin_amdgcn_rcpf(low32(keep[10] + c2[2]));
      store4_sc1(u32x4{pkw((keep[0] + c0[0]) * sc, (keep[1] + c0[1]) * sc), pkw((keep[2] + c0[2]) * sc, (keep[3] + c0[3]) * sc),
                       pkw((keep[4] + c1[0]) * sc, (keep[5] + c1[1]) * sc), pkw((keep[6] + c1[2]) * sc, (keep[7] + c1[3]) * sc)},
                 rs_ws, lane * 16, ob + (2 * c + FG) * 1024);
      if constexpr (!FG) {
        const float s1 = gate * __builtin_amdgcn_rcpf(send[10] + ce[2]);
        if (h == 0) store2_sc1(u32x2{pkw((keep[8] + c2[0]) * sc, (keep[9] + c2[1]) * sc), pkw((send[8] + ce[0]) * s1, (send[9] + ce[1]) * s1)},
                               rs_ws, l31 * 8, ob + 32768 + c * 256);
      }
    };
    if (fg) fin.template operator()<1>(O1, O0); else fin.template operator()<0>(O0, O1);
  }
  STAMP(9);
  if (bi >= 24) {
    if (b == 255 && w == 0) {
#pragma unroll
      for (int m = 0; m < 4; ++m) spin_flags(P.ws, WS_PFLAG + ((m * 4 + (lane >> 4)) * 32 + (lane & 15)) * 4, true, tag);
      set_flag(rs_ws, WS_EPOCH, tag);
    }
    return;
  }

  {
    pred.template operator()<10>(); pred.template operator()<11>();
    Unroll<WOPRE, 27>::run([&]<int I>() { wo_read.template operator()<I>(); });
    u32x4 of[9];
    const unsigned oo = WS_O + (2 * T2 + tg2) * 36864;
#pragma unroll 1
    for (unsigned it = 0; it < (1u << 22); ++it) {
      u32x2 r0, r1;
#pragma unroll
      for (int i = 0; i < 8; ++i) aload4_sc1(of[i], rs_ws, lane * 16, oo + (8 * kq + i) * 1024);
      aload2_sc1(r0, rs_ws, l31 * 8 + h * 512, oo + 32768 + kq * 1024);
      aload2_sc1(r1, rs_ws, l31 * 8 + h * 512, oo + 32768 + kq * 1024 + 256);
      asm volatile("s_cmp_lg_u32 %0, 0\n\ts_cbranch_scc1 1f\n\ts_waitcnt lgkmcnt(0)\n\ts_barrier\n1:" :: "s"(it) : "scc", "memory");
      wait_vm<0>();
      TIE(r0); TIE(r1);
      of[8] = u32x4{r0[0], r0[1], r1[0], r1[1]};
      unsigned m = 0;
#pragma unroll
      for (int i = 0; i < 9; ++i) { if (i < 8) TIE(of[i]); m = umax(umax(m, umax(of[i][0], of[i][1])), umax(of[i][2], of[i][3])); }
      if (__all(m != ~0u)) break;
    }
    STAMP(10);
#pragma unroll
    for (int i = 0; i < 9; ++i) {
      pm[0] = mfma32(wo[i], asfrag(of[i]), pm[0]); pm[1] = mfma32(wo[9 + i], asfrag(of[i]), pm[1]); pm[2] = mfma32(wo[18 + i], asfrag(of[i]), pm[2]);
    }
    const unsigned ra = kq * 25600 + (32 * tg2 + l31) * 400 + 16 * h;
#pragma unroll
    for (int g4 = 0; g4 < 4; ++g4) {
      lds_st<f32x4>(sm, ra + g4 * 32, f32x4{pm[0][4 * g4], pm[0][4 * g4 + 1], pm[0][4 * g4 + 2], pm[0][4 * g4 + 3]});
      lds_st<f32x4>(sm, ra + 128 + g4 * 32, f32x4{pm[1][4 * g4], pm[1][4 * g4 + 1], pm[1][4 * g4 + 2], pm[1][4 * g4 + 3]});
      lds_st<f32x4>(sm, ra + 256 + g4 * 32, f32x4{pm[2][4 * g4], pm[2][4 * g4 + 1], pm[2][4 * g4 + 2], pm[2][4 * g4 + 3]});
    }
    sync_lds();
    const u32x4 rs_out = mkrs((char*)P.out + 64 * T2 * 1152, 64 * 1152);
#pragma unroll
    for (int m = 0; m < 2; ++m) {
      if (m == 1 && w >= 4) break;
      const unsigned idx = tid + m * 512, t = idx / 12, cg = idx - t * 12;
      const unsigned a = t * 400 + cg * 32;
      const f32x4 s0 = (lds_ld<f32x4>(sm, a) + lds_ld<f32x4>(sm, a + 25600)) + (lds_ld<f32x4>(sm, a + 51200) + lds_ld<f32x4>(sm, a + 76800));
      const f32x4 s1 = (lds_ld<f32x4>(sm, a + 16) + lds_ld<f32x4>(sm, a + 25616)) + (lds_ld<f32x4>(sm, a + 51216) + lds_ld<f32x4>(sm, a + 76816));
      const u32x4 v = {cvtpk(s0[0], s0[1]), cvtpk(s0[2], s0[3]), cvtpk(s1[0], s1[1]), cvtpk(s1[2], s1[3])};
      asm volatile("buffer_store_dwordx4 %0, %1, %2, %3 offen " OUTMOD "\n\ts_nop 1" :: "v"(v), "v"(t * 1152 + cg * 16), "s"(rs_out), "s"(nb * 192) : "memory");
    }
  }
  STAMP(11);
  }
}

extern "C" void kernel_launch(void* const* d_in, const int* in_sizes, int n_in, void* d_out, int out_size,
                              void* d_ws, size_t ws_size, hipStream_t stream) {
  Params P;
  P.x = d_in[0]; P.wq = d_in[1]; P.wk = d_in[2]; P.wv = d_in[3]; P.wo = d_in[4];
  P.w1 = d_in[6]; P.b1 = d_in[7]; P.w2 = d_in[8]; P.b2 = d_in[9]; P.gw = d_in[10];
  P.out = d_out; P.ws = d_ws;
  fused_kernel<<<256, 512, 0, stream>>>(P);
}
```
